```python
import torch

def _softmax(x, axis=-1):
    return torch.softmax(x, dim=axis)

def _gelu(x):
    return torch.nn.functional.gelu(x, approximate='tanh')

def _silu(x):
    return torch.nn.functional.silu(x)

def _sigmoid(x):
    return torch.sigmoid(x)

def _layernorm(x, axis=-1, eps=1e-05):
    m = x.mean(dim=axis, keepdim=True)
    v = x.var(dim=axis, keepdim=True, unbiased=False)
    return (x - m) / torch.sqrt(v + eps)

def _scatter_add(tgt, idx, src, axis=0):
    return tgt.clone().index_add_(axis, idx, src)

def _segment_sum(src, seg, n):
    out = torch.zeros((n,) + src.shape[1:], dtype=src.dtype)
    return out.index_add_(0, seg, src)

def _pad(x, pw):
    flat = tuple((v for pair in reversed(pw) for v in pair))
    return torch.nn.functional.pad(x, flat)
kernel_name = 'parallel_sum__linear_attn_block__mixer_gmlp__prompt'

def setup_inputs():

    _w = lambda a: (lambda t: t.to(torch.bfloat16) if t.is_floating_point() else t)(torch.as_tensor(a))

    def _A_setup():
        B, S, D = (1, 2048, 640)
        return {'x': _w((lambda *s: torch.randn(*s, dtype=torch.bfloat16) * 0.05)(B, S, D)), 'wqkv': _w((lambda *s: torch.randn(*s, dtype=torch.bfloat16) * 0.8)(D, 3 * D)), 'wo': _w((lambda *s: torch.randn(*s, dtype=torch.bfloat16) * 0.2)(D, D))}
    _A_in = _A_setup()

    def _B_setup():
        B, S, D, F = (1, 2048, 640, 1536)
        Fh = F // 2
        return {'x': _w((lambda *s: torch.randn(*s, dtype=torch.bfloat16) * 0.05)(B, S, D)), 'w_in': _w((lambda *s: torch.randn(*s, dtype=torch.bfloat16) * 0.4)(D, 2 * Fh)), 'w_sp': _w(0.01 * (lambda *s: torch.randn(*s, dtype=torch.bfloat16) * 0.4)(S, S)), 'b_sp': _w(1.0 + 0.01 * (lambda *s: torch.randn(*s, dtype=torch.bfloat16) * 0.05)(S)), 'w_out': _w((lambda *s: torch.randn(*s, dtype=torch.bfloat16) * 0.05)(Fh, D))}
    _B_in = _B_setup()
    return {'A_x': _A_in['x'], 'A_wqkv': _A_in['wqkv'], 'A_wo': _A_in['wo'], 'B_x': _B_in['x'], 'B_w_in': _B_in['w_in'], 'B_w_sp': _B_in['w_sp'], 'B_b_sp': _B_in['b_sp'], 'B_w_out': _B_in['w_out']}

def reference(A_x, A_wqkv, A_wo, B_x, B_w_in, B_w_sp, B_b_sp, B_w_out):

    def _A_ref(x, wqkv, wo):
        B, S, D = x.shape
        H = 32
        Dh = D // H
        qkv = (lambda x, *d: x.permute(*d))((x @ wqkv).reshape(B, S, 3, H, Dh), 2, 0, 3, 1, 4)
        q, k, v = (qkv[0], qkv[1], qkv[2])
        phi = lambda t: torch.where(t > 0, t + 1, torch.exp(t)) + 1e-06
        pq, pk = (phi(q), phi(k))
        kv = torch.einsum('bhsd,bhse->bhde', pk, v)
        z = torch.einsum('bhsd->bhd', pk)
        num = torch.einsum('bhqd,bhde->bhqe', pq, kv)
        den = torch.einsum('bhqd,bhd->bhq', pq, z)[..., None] + 1e-06
        o = num / den
        return (lambda x, *d: x.permute(*d))(o, 0, 2, 1, 3).reshape(B, S, D) @ wo

    def _B_ref(x, w_in, w_sp, b_sp, w_out):
        B, S, D = x.shape
        F = w_in.shape[-1]
        Fh = F // 2
        z = _gelu(torch.einsum('bsd,df->bsf', x, w_in))
        z1 = z[..., :Fh].clone()
        z2 = _layernorm(z[..., Fh:], axis=-1)
        g = torch.einsum('ts,bsf->btf', w_sp, z2) + b_sp[None, :, None]
        return torch.einsum('bsf,fd->bsd', z1 * g, w_out)
    x = A_x
    xn = _layernorm(x, axis=-1)
    return x + _A_ref(xn, A_wqkv, A_wo) + _B_ref(xn, B_w_in, B_w_sp, B_b_sp, B_w_out)
```

```cpp
#include <hip/hip_runtime.h>
#include <hip/hip_bf16.h>
#include <stdint.h>

typedef short bf16x8 __attribute__((ext_vector_type(8)));
typedef short s16x4 __attribute__((ext_vector_type(4)));
typedef float f32x4 __attribute__((ext_vector_type(4)));
typedef float f32x2 __attribute__((ext_vector_type(2)));
typedef unsigned u32x4 __attribute__((ext_vector_type(4)));
typedef unsigned u32x2 __attribute__((ext_vector_type(2)));
typedef __bf16 bf16x2_t __attribute__((ext_vector_type(2)));
typedef unsigned short u16;

#define LAS __attribute__((address_space(3)))
#define DI __device__ __forceinline__

constexpr int S_ = 2048, D_ = 640, H_ = 32, DH_ = 20, F_ = 768, KA_ = D_ + F_;

constexpr size_t WS_Z2 = 0;
constexpr size_t WS_PQ = WS_Z2 + (size_t)S_ * F_ * 2;
constexpr size_t WS_UA = WS_PQ + (size_t)S_ * D_ * 2;
constexpr size_t WS_M2 = WS_UA + (size_t)S_ * KA_ * 2;
constexpr size_t WS_KVP = WS_M2 + (size_t)D_ * D_ * 2;
constexpr size_t WS_Z2ST = WS_KVP + (size_t)16 * 32 * 420 * 4;
constexpr size_t WS_XST = WS_Z2ST + (size_t)S_ * 4 * 2 * 4;

DI unsigned pack_bf16(float a, float b) {
  f32x2 v = {a, b};
  bf16x2_t r = __builtin_convertvector(v, bf16x2_t);
  return __builtin_bit_cast(unsigned, r);
}
DI float bf_round(float a) { return (float)(__bf16)a; }
DI float bf_lo(unsigned w) { return __builtin_bit_cast(float, w << 16); }
DI float bf_hi(unsigned w) { return __builtin_bit_cast(float, w & 0xffff0000u); }
DI float phi_f(float t) { return (t > 0.f ? t + 1.f : __expf(t)) + 1e-6f; }
DI float gelu_f(float x) {
  float u = 0.7978845608028654f * (x + 0.044715f * x * x * x);
  return x * __builtin_amdgcn_rcpf(1.f + __expf(-2.f * u));
}
DI f32x4 mfma16(bf16x8 a, bf16x8 b, f32x4 c) { return __builtin_amdgcn_mfma_f32_16x16x32_bf16(a, b, c, 0, 0, 0); }
DI bf16x8 lds_b128(unsigned addr) { return *(LAS bf16x8*)(uintptr_t)addr; }
DI bf16x8 lds_tr(unsigned a0, unsigned a1) {
  s16x4 lo = __builtin_amdgcn_ds_read_tr16_b64_v4i16((LAS s16x4*)(uintptr_t)a0);
  s16x4 hi = __builtin_amdgcn_ds_read_tr16_b64_v4i16((LAS s16x4*)(uintptr_t)a1);
  return __builtin_shufflevector(lo, hi, 0, 1, 2, 3, 4, 5, 6, 7);
}
DI void glds16(const void* g, unsigned lds_addr) {
  __builtin_amdgcn_global_load_lds((const __attribute__((address_space(1))) unsigned*)g, (LAS unsigned*)(uintptr_t)lds_addr, 16, 0, 0);
}
#define WAIT_VM(n) asm volatile("s_waitcnt vmcnt(%0)" ::"i"(n) : "memory")
#define WAIT_LGKM0() asm volatile("s_waitcnt lgkmcnt(0)" ::: "memory")
#define BARRIER() __builtin_amdgcn_s_barrier()

template <int NB>
DI void bslot_kn(int slot, int& k, int& n) {
  int st = slot >> 4, w = slot & 15;
  int kb = st / NB, nb = st - kb * NB;
  k = kb * 8 + ((w >> 1) ^ ((kb & 1) << 2));
  n = nb * 16 + (w & 1) * 8;
}

struct Params {
  const u16* x;
  const u16* wqkv;
  const u16* wo;
  const u16* w_in;
  const u16* w_sp;
  const u16* b_sp;
  const u16* w_out;
  u16* out;
  char* ws;
};

constexpr int S1_STAGE = 16384 + 24576;
constexpr int S1_LDS = 3 * S1_STAGE;

template <int TYPE>
DI void stage1_body(const Params& p, int rg, int ct, unsigned lds0) {
  constexpr int BN = TYPE == 2 ? 192 : 160;
  constexpr int NB = BN / 16;
  constexpr int NF = NB / 2;
  constexpr int BI = (64 * BN * 2) / 4096;
  constexpr int G = 4 + BI;
  constexpr int NT = D_ / 64;
  const int tid = threadIdx.x, lane = tid & 63;
  const int wid = __builtin_amdgcn_readfirstlane(tid >> 6);
  const int wr = wid >> 1, wc = wid & 1;
  const int fr = lane & 15, g = lane >> 4;

  const u16* Bm = TYPE == 2 ? p.w_in : p.wqkv;
  constexpr int ldb = TYPE == 2 ? 2 * F_ : 3 * D_;

  const u16* asrc[4];
#pragma unroll
  for (int i = 0; i < 4; ++i) {
    int u = i * 4 + wid;
    int row = u * 8 + (lane >> 3), c = (lane & 7) ^ ((row >> 1) & 7);
    asrc[i] = p.x + (size_t)(rg * 128 + row) * D_ + c * 8;
  }
  const u16* bsrc[BI];
#pragma unroll
  for (int i = 0; i < BI; ++i) {
    int u = i * 4 + wid, k, n;
    bslot_kn<NB>(u * 64 + lane, k, n);
    int col;
    if (TYPE == 0) {
      int h2 = n / 80, w = n - h2 * 80;
      int h0 = ct * 4 + h2 * 2;
      col = (w < 40) ? (D_ + h0 * DH_ + w) : (2 * D_ + h0 * DH_ + (w - 40));
    } else if (TYPE == 1) {
      col = ct * 160 + n;
    } else {
      col = F_ + ct * 192 + n;
    }
    bsrc[i] = Bm + (size_t)k * ldb + col;
  }
  auto issue = [&](int t, int buf) {
    unsigned base = lds0 + buf * S1_STAGE;
#pragma unroll
    for (int i = 0; i < 4; ++i) glds16(asrc[i] + t * 64, base + (i * 4 + wid) * 1024);
#pragma unroll
    for (int i = 0; i < BI; ++i) glds16(bsrc[i] + (size_t)t * 64 * ldb, base + 16384 + (i * 4 + wid) * 1024);
  };

  unsigned abase[2], bbase[2];
  {
    int x7 = (fr >> 1) & 7;
#pragma unroll
    for (int ks = 0; ks < 2; ++ks) abase[ks] = lds0 + (wr * 64 + fr) * 128 + (((ks * 4 + g) ^ x7) << 4);
    int q = fr >> 2, pp = fr & 3;
#pragma unroll
    for (int t = 0; t < 2; ++t)
      bbase[t] = lds0 + 16384 + (g * NB + wc * NF) * 256 + (((4 * t + q) ^ ((g & 1) << 2)) << 5) + pp * 8;
  }

  f32x4 acc[4][NF];
  f32x4 rs[4], sq[4], cs[NF];
#pragma unroll
  for (int m = 0; m < 4; ++m) {
    rs[m] = f32x4{0, 0, 0, 0};
    sq[m] = f32x4{0, 0, 0, 0};
#pragma unroll
    for (int n = 0; n < NF; ++n) acc[m][n] = f32x4{0, 0, 0, 0};
  }
#pragma unroll
  for (int n = 0; n < NF; ++n) cs[n] = f32x4{0, 0, 0, 0};
  const short one = 0x3f80;
  const bf16x8 ones = {one, one, one, one, one, one, one, one};

  issue(0, 0);
  issue(1, 1);
  int buf = 0;
#pragma unroll 1
  for (int t = 0; t < NT; ++t) {
    if (t < NT - 1) WAIT_VM(G); else WAIT_VM(0);
    WAIT_LGKM0();
    BARRIER();
    if (t + 2 < NT) issue(t + 2, buf == 0 ? 2 : buf - 1);
    unsigned sb = buf * S1_STAGE;
#pragma unroll
    for (int ks = 0; ks < 2; ++ks) {
      bf16x8 af[4], bq[NF];
#pragma unroll
      for (int m = 0; m < 4; ++m) af[m] = lds_b128(abase[ks] + sb + m * 2048);
#pragma unroll
      for (int n = 0; n < NF; ++n)
        bq[n] = lds_tr(bbase[0] + sb + ks * 4 * NB * 256 + n * 256, bbase[1] + sb + ks * 4 * NB * 256 + n * 256);
#pragma unroll
      for (int m = 0; m < 4; ++m)
#pragma unroll
        for (int n = 0; n < NF; ++n)
          acc[m][n] = TYPE == 0 ? mfma16(af[m], bq[n], acc[m][n]) : mfma16(bq[n], af[m], acc[m][n]);
      if (ks == wc) {
#pragma unroll
        for (int m = 0; m < 4; ++m) {
          rs[m] = mfma16(af[m], ones, rs[m]);
          sq[m] = mfma16(af[m], af[m], sq[m]);
        }
      }
      if (ks == wr) {
#pragma unroll
        for (int n = 0; n < NF; ++n) cs[n] = mfma16(ones, bq[n], cs[n]);
      }
    }
    buf = buf == 2 ? 0 : buf + 1;
  }
  WAIT_LGKM0();
  BARRIER();
  LAS float* st_rs = (LAS float*)(uintptr_t)lds0;
  LAS float* st_sq = st_rs + 256;
  LAS float* st_cs = st_sq + 256;
  LAS float* st_x = st_cs + 384;
#pragma unroll
  for (int m = 0; m < 4; ++m) {
    if (fr == 0) {
#pragma unroll
      for (int r = 0; r < 4; ++r) st_rs[wc * 128 + wr * 64 + m * 16 + g * 4 + r] = rs[m][r];
    }
    if ((fr >> 2) == g) {
      int r = fr & 3;
      float v = r == 0 ? sq[m][0] : r == 1 ? sq[m][1] : r == 2 ? sq[m][2] : sq[m][3];
      st_sq[wc * 128 + wr * 64 + m * 16 + fr] = v;
    }
  }
  if (g == 0) {
#pragma unroll
    for (int n = 0; n < NF; ++n) st_cs[wr * 192 + wc * NF * 16 + n * 16 + fr] = cs[n][0];
  }
  __syncthreads();
  auto row_stat = [&](int row, float& mean, float& rstd) {
    float s1 = st_rs[row] + st_rs[128 + row];
    float s2 = st_sq[row] + st_sq[128 + row];
    mean = s1 * (1.f / D_);
    float var = fmaxf(s2 * (1.f / D_) - mean * mean, 0.f);
    rstd = __builtin_amdgcn_rsqf(var + 1e-5f);
  };

  if (TYPE == 0) {
    float csv[NF];
#pragma unroll
    for (int n = 0; n < NF; ++n) csv[n] = st_cs[wc * NF * 16 + n * 16 + fr] + st_cs[192 + wc * NF * 16 + n * 16 + fr];
    bf16x8 xf[2][NF];
#pragma unroll
    for (int m = 0; m < 4; ++m) {
      float mean[4], rstd[4];
#pragma unroll
      for (int r = 0; r < 4; ++r) row_stat(wr * 64 + m * 16 + g * 4 + r, mean[r], rstd[r]);
#pragma unroll
      for (int n = 0; n < NF; ++n) {
        float v[4];
        bool isk = (n * 16 + fr) < 40;
#pragma unroll
        for (int r = 0; r < 4; ++r) {
          float y = bf_round(rstd[r] * (acc[m][n][r] - mean[r] * csv[n]));
          v[r] = isk ? phi_f(y) : y;
        }
        unsigned w0 = pack_bf16(v[0], v[1]), w1 = pack_bf16(v[2], v[3]);
        u32x4 t4 = __builtin_bit_cast(u32x4, xf[m >> 1][n]);
        t4[(m & 1) * 2] = w0;
        t4[(m & 1) * 2 + 1] = w1;
        xf[m >> 1][n] = __builtin_bit_cast(bf16x8, t4);
      }
    }
    constexpr int PI[7] = {0, 0, 1, 1, 1, 2, 2};
    constexpr int PJ[7] = {2, 3, 2, 3, 4, 3, 4};
    LAS float* kvl = st_x + wr * (4 * 420) + wc * (2 * 420);
#pragma unroll
    for (int q = 0; q < 7; ++q) {
      f32x4 d = {0, 0, 0, 0};
      d = mfma16(xf[0][PI[q]], xf[0][PJ[q]], d);
      d = mfma16(xf[1][PI[q]], xf[1][PJ[q]], d);
      int cj = PJ[q] * 16 + fr - 40;
#pragma unroll
      for (int r = 0; r < 4; ++r) {
        int ci = PI[q] * 16 + g * 4 + r;
        if (ci < 40 && cj >= 0) {
          int hi = ci >= 20, hj = cj >= 20;
          if (hi == hj) kvl[hi * 420 + (ci - hi * 20) * 20 + (cj - hj * 20)] = d[r];
        }
      }
    }
#pragma unroll
    for (int i = 0; i < 3; ++i) {
      f32x4 d = {0, 0, 0, 0};
      d = mfma16(xf[0][i], ones, d);
      d = mfma16(xf[1][i], ones, d);
      if (fr == 0) {
#pragma unroll
        for (int r = 0; r < 4; ++r) {
          int ci = i * 16 + g * 4 + r;
          if (ci < 40) {
            int hi = ci >= 20;
            kvl[hi * 420 + 400 + (ci - hi * 20)] = d[r];
          }
        }
      }
    }
    __syncthreads();
    float* kvp = (float*)(p.ws + WS_KVP) + ((size_t)rg * 32 + ct * 4) * 420;
    for (int i = tid; i < 4 * 420; i += 256) kvp[i] = st_x[i] + st_x[4 * 420 + i];
  } else {
    f32x4 csv[NF];
#pragma unroll
    for (int n = 0; n < NF; ++n) {
      int c0 = wc * NF * 16 + n * 16 + g * 4;
      csv[n] = *(LAS f32x4*)(st_cs + c0) + *(LAS f32x4*)(st_cs + 192 + c0);
    }
#pragma unroll
    for (int m = 0; m < 4; ++m) {
      int rl = wr * 64 + m * 16 + fr;
      int s = rg * 128 + rl;
      float mean, rstd;
      row_stat(rl, mean, rstd);
      if (TYPE == 1) {
        u16* dst = (u16*)(p.ws + WS_PQ) + (size_t)s * D_ + ct * 160 + wc * NF * 16 + g * 4;
#pragma unroll
        for (int n = 0; n < NF; ++n) {
          float v[4];
#pragma unroll
          for (int r = 0; r < 4; ++r) v[r] = phi_f(bf_round(rstd * (acc[m][n][r] - mean * csv[n][r])));
          u32x2 w = {pack_bf16(v[0], v[1]), pack_bf16(v[2], v[3])};
          *(u32x2*)(dst + n * 16) = w;
        }
      } else {
        u16* dst = (u16*)(p.ws + WS_Z2) + (size_t)s * F_ + ct * 192 + wc * NF * 16 + g * 4;
        float zs = 0.f;
        float zv[NF][4];
#pragma unroll
        for (int n = 0; n < NF; ++n) {
#pragma unroll
          for (int r = 0; r < 4; ++r) {
            zv[n][r] = bf_round(gelu_f(bf_round(rstd * (acc[m][n][r] - mean * csv[n][r]))));
            zs += zv[n][r];
          }
          u32x2 w = {pack_bf16(zv[n][0], zv[n][1]), pack_bf16(zv[n][2], zv[n][3])};
          *(u32x2*)(dst + n * 16) = w;
        }
        zs += __shfl_xor(zs, 16);
        zs += __shfl_xor(zs, 32);
        float zm = zs * (1.f / 96.f);
        float m2 = 0.f;
#pragma unroll
        for (int n = 0; n < NF; ++n)
#pragma unroll
          for (int r = 0; r < 4; ++r) {
            float d = zv[n][r] - zm;
            m2 += d * d;
          }
        m2 += __shfl_xor(m2, 16);
        m2 += __shfl_xor(m2, 32);
        if (g == 0) {
          st_x[(wc * 128 + rl) * 2] = zm;
          st_x[(wc * 128 + rl) * 2 + 1] = m2;
        }
        if (ct == 0 && wc == 0 && g == 0) {
          f32x2 o = {mean, rstd};
          *(f32x2*)((float*)(p.ws + WS_XST) + (size_t)s * 2) = o;
        }
      }
    }
    if (TYPE == 2) {
      __syncthreads();
      if (tid < 128) {
        float ma = st_x[tid * 2], qa = st_x[tid * 2 + 1];
        float mb = st_x[(128 + tid) * 2], qb = st_x[(128 + tid) * 2 + 1];
        float dl = mb - ma;
        f32x2 o = {0.5f * (ma + mb), qa + qb + dl * dl * 48.f};
        *(f32x2*)((float*)(p.ws + WS_Z2ST) + ((size_t)(rg * 128 + tid) * 4 + ct) * 2) = o;
      }
    }
  }
}

__global__ __launch_bounds__(256, 2) void stage1_kernel(Params p) {
  extern __shared__ __attribute__((aligned(16))) char shm[];
  unsigned lds0 = (unsigned)(uintptr_t)(LAS char*)shm;
  int b = blockIdx.x, xcd = b & 7, j = b >> 3;
  int rg = (xcd & 1) * 8 + (j & 7);
  int cq = xcd >> 1, ci = j >> 3;
  if (ci < 2) stage1_body<0>(p, rg, cq * 2 + ci, lds0);
  else if (ci == 2) stage1_body<1>(p, rg, cq, lds0);
  else stage1_body<2>(p, rg, cq, lds0);
}

constexpr int S2_STAGE = 8192 + 12288;
constexpr int S2_NBUF = 5;
constexpr int S2_P = 3;
constexpr int S2_OFF_ST = S2_NBUF * S2_STAGE;
constexpr int S2_OFF_X = S2_OFF_ST + S_ * 8;
constexpr int S2_LDS = S2_OFF_X + 8192;
constexpr int S2_NT1 = D_ / 64, S2_NT = S2_NT1 + S_ / 64;

__global__ __launch_bounds__(512, 2) void stage2_kernel(Params p) {
  extern __shared__ __attribute__((aligned(16))) char shm[];
  const unsigned lds0 = (unsigned)(uintptr_t)(LAS char*)shm;
  const int b = blockIdx.x, xcd = b & 7, j = b >> 3;
  const int rg = (xcd & 3) * 8 + (j & 7);
  const int cb = (xcd >> 2) * 4 + (j >> 3);
  const int tid = threadIdx.x, lane = tid & 63;
  const int wid = __builtin_amdgcn_readfirstlane(tid >> 6);
  constexpr int NB = 6;

  if (wid < 4) {
    const int wr = wid >> 1, wc = wid & 1;
    const int fr = lane & 15, g = lane >> 4;
    unsigned aoff[2];
#pragma unroll
    for (int i = 0; i < 2; ++i) {
      int u = i * 4 + wid;
      int row = u * 8 + (lane >> 3), c = (lane & 7) ^ ((row >> 1) & 7);
      aoff[i] = row * 65536u + c * 8;
    }
    auto issueA = [&](int t) {
      unsigned base = lds0 + (t % S2_NBUF) * S2_STAGE;
#pragma unroll
      for (int i = 0; i < 2; ++i) {
        int row = aoff[i] >> 16, c8 = aoff[i] & 0xffff;
        const u16* src = t < S2_NT1 ? p.x + (size_t)(rg * 64 + row) * D_ + t * 64 + c8
                                    : p.w_sp + (size_t)(rg * 64 + row) * S_ + (t - S2_NT1) * 64 + c8;
        glds16(src, base + (i * 4 + wid) * 1024);
      }
    };
#pragma unroll
    for (int t = 0; t <= S2_P; ++t) issueA(t);

    {
      const f32x4* zst = (const f32x4*)(p.ws + WS_Z2ST);
      LAS f32x2* st = (LAS f32x2*)(uintptr_t)(lds0 + S2_OFF_ST);
#pragma unroll
      for (int i = 0; i < 8; ++i) {
        int s = i * 256 + tid;
        f32x4 a = zst[s * 2], c = zst[s * 2 + 1];
        float mean = 0.25f * (a[0] + a[2] + c[0] + c[2]);
        float d0 = a[0] - mean, d1 = a[2] - mean, d2 = c[0] - mean, d3 = c[2] - mean;
        float m2 = a[1] + a[3] + c[1] + c[3] + 192.f * (d0 * d0 + d1 * d1 + d2 * d2 + d3 * d3);
        float rstd = __builtin_amdgcn_rsqf(m2 * (1.f / F_) + 1e-5f);
        st[s] = f32x2{rstd, -mean * rstd};
      }
      LAS float* sx = (LAS float*)(uintptr_t)(lds0 + S2_OFF_X);
      const float* kvp = (const float*)(p.ws + WS_KVP);
      const int h = b >> 3, cc = b & 7;
      for (int i = tid; i < 480; i += 256) {
        int off = i < 400 ? h * 420 + i : (cb * 4 + (i - 400) / 20) * 420 + 400 + (i - 400) % 20;
        float s = 0.f;
#pragma unroll
        for (int q = 0; q < 16; ++q) s += kvp[(size_t)q * 32 * 420 + off];
        sx[i] = s;
      }
      for (int i = tid; i < 200; i += 256) {
        int e = i / 10, c8 = i - e * 10;
        u32x4 w = *(const u32x4*)(p.wo + (size_t)(h * DH_ + e) * D_ + cc * 80 + c8 * 8);
#pragma unroll
        for (int q = 0; q < 4; ++q) {
          sx[480 + e * 80 + c8 * 8 + q * 2] = bf_lo(w[q]);
          sx[480 + e * 80 + c8 * 8 + q * 2 + 1] = bf_hi(w[q]);
        }
      }
    }
    WAIT_LGKM0();
    BARRIER();
    {
      LAS float* sx = (LAS float*)(uintptr_t)(lds0 + S2_OFF_X);
      const int h = b >> 3, cc = b & 7;
      u16* M2 = (u16*)(p.ws + WS_M2);
      for (int i = tid; i < 800; i += 256) {
        int d = i / 40, c2 = (i - d * 40) * 2;
        float a0 = 0.f, a1 = 0.f;
#pragma unroll
        for (int e = 0; e < DH_; ++e) {
          float kvv = sx[d * 20 + e];
          a0 += kvv * sx[480 + e * 80 + c2];
          a1 += kvv * sx[480 + e * 80 + c2 + 1];
        }
        *(unsigned*)(M2 + (size_t)(h * DH_ + d) * D_ + cc * 80 + c2) = pack_bf16(a0, a1);
      }
      int r = tid >> 2, hj = tid & 3;
      const u16* src = (const u16*)(p.ws + WS_PQ) + (size_t)(rg * 64 + r) * D_ + cb * 80 + hj * 20;
      u16* dst = (u16*)(p.ws + WS_UA) + (size_t)(rg * 64 + r) * KA_ + cb * 80 + hj * 20;
      u32x2 w[5];
#pragma unroll
      for (int q = 0; q < 5; ++q) w[q] = *(const u32x2*)(src + q * 4);
      float den = 0.f;
#pragma unroll
      for (int q = 0; q < 5; ++q) {
        den += bf_lo(w[q][0]) * sx[400 + hj * 20 + q * 4] + bf_hi(w[q][0]) * sx[400 + hj * 20 + q * 4 + 1] +
               bf_lo(w[q][1]) * sx[400 + hj * 20 + q * 4 + 2] + bf_hi(w[q][1]) * sx[400 + hj * 20 + q * 4 + 3];
      }
      float inv = 1.f / (den + 1e-6f);
#pragma unroll
      for (int q = 0; q < 5; ++q) {
        u32x2 o = {pack_bf16(bf_lo(w[q][0]) * inv, bf_hi(w[q][0]) * inv), pack_bf16(bf_lo(w[q][1]) * inv, bf_hi(w[q][1]) * inv)};
        *(u32x2*)(dst + q * 4) = o;
      }
    }
    WAIT_VM(0);

    unsigned abase[2], bbase[2];
    {
      int x7 = (fr >> 1) & 7;
#pragma unroll
      for (int ks = 0; ks < 2; ++ks) abase[ks] = lds0 + (wr * 32 + fr) * 128 + (((ks * 4 + g) ^ x7) << 4);
      int q = fr >> 2, pp = fr & 3;
#pragma unroll
      for (int t = 0; t < 2; ++t)
        bbase[t] = lds0 + 8192 + (g * NB + wc * 3) * 256 + (((4 * t + q) ^ ((g & 1) << 2)) << 5) + pp * 8;
    }
    f32x4 acc1[2][3], acc2[2][3], cs[3];
#pragma unroll
    for (int m = 0; m < 2; ++m)
#pragma unroll
      for (int n = 0; n < 3; ++n) { acc1[m][n] = f32x4{0, 0, 0, 0}; acc2[m][n] = f32x4{0, 0, 0, 0}; }
#pragma unroll
    for (int n = 0; n < 3; ++n) cs[n] = f32x4{0, 0, 0, 0};
    const short one = 0x3f80;
    const bf16x8 ones = {one, one, one, one, one, one, one, one};

    BARRIER();
    int buf = 0;
#pragma unroll 1
    for (int t = 0; t < S2_NT; ++t) {
      if (t + 1 + S2_P < S2_NT) issueA(t + 1 + S2_P);
      unsigned sb = buf * S2_STAGE;
      if (t < S2_NT1) {
#pragma unroll
        for (int ks = 0; ks < 2; ++ks) {
          bf16x8 af[2], bq[3];
#pragma unroll
          for (int m = 0; m < 2; ++m) af[m] = lds_b128(abase[ks] + sb + m * 2048);
#pragma unroll
          for (int n = 0; n < 3; ++n)
            bq[n] = lds_tr(bbase[0] + sb + ks * 4 * NB * 256 + n * 256, bbase[1] + sb + ks * 4 * NB * 256 + n * 256);
#pragma unroll
          for (int m = 0; m < 2; ++m)
#pragma unroll
            for (int n = 0; n < 3; ++n) acc1[m][n] = mfma16(bq[n], af[m], acc1[m][n]);
#pragma unroll
          for (int n = 0; n < 3; ++n) cs[n] = mfma16(bq[n], ones, cs[n]);
        }
      } else {
#pragma unroll
        for (int ks = 0; ks < 2; ++ks) {
          bf16x8 af[2], bq[3];
#pragma unroll
          for (int m = 0; m < 2; ++m) af[m] = lds_b128(abase[ks] + sb + m * 2048);
#pragma unroll
          for (int n = 0; n < 3; ++n)
            bq[n] = lds_tr(bbase[0] + sb + ks * 4 * NB * 256 + n * 256, bbase[1] + sb + ks * 4 * NB * 256 + n * 256);
#pragma unroll
          for (int m = 0; m < 2; ++m)
#pragma unroll
            for (int n = 0; n < 3; ++n) acc2[m][n] = mfma16(bq[n], af[m], acc2[m][n]);
        }
      }
      if (t + 1 + S2_P < S2_NT) WAIT_VM(2 * S2_P); else WAIT_VM(0);
      WAIT_LGKM0();
      BARRIER();
      buf = buf == S2_NBUF - 1 ? 0 : buf + 1;
    }
#pragma unroll
    for (int m = 0; m < 2; ++m) {
      int s = rg * 64 + wr * 32 + m * 16 + fr;
      f32x2 xs = *(const f32x2*)((const float*)(p.ws + WS_XST) + (size_t)s * 2);
      float bias = bf_lo(p.b_sp[s]);
      u16* dst = (u16*)(p.ws + WS_UA) + (size_t)s * KA_ + D_ + cb * 96 + wc * 48 + g * 4;
#pragma unroll
      for (int n = 0; n < 3; ++n) {
        float v[4];
#pragma unroll
        for (int r = 0; r < 4; ++r) {
          float z1 = bf_round(gelu_f(bf_round(xs[1] * (acc1[m][n][r] - xs[0] * cs[n][r]))));
          float gg = bf_round(bf_round(acc2[m][n][r]) + bias);
          v[r] = z1 * gg;
        }
        u32x2 w = {pack_bf16(v[0], v[1]), pack_bf16(v[2], v[3])};
        *(u32x2*)(dst + n * 16) = w;
      }
    }
  } else {
    const int pt = tid - 256;
    int kk[3], nn[3];
#pragma unroll
    for (int i = 0; i < 3; ++i) bslot_kn<NB>(i * 256 + pt, kk[i], nn[i]);
    const u16* z2 = (const u16*)(p.ws + WS_Z2);
    auto loadB = [&](int t, u32x4 (&r)[3]) {
#pragma unroll
      for (int i = 0; i < 3; ++i) {
        const u16* src = t < S2_NT1 ? p.w_in + (size_t)(t * 64 + kk[i]) * (2 * F_) + cb * 96 + nn[i]
                                    : z2 + (size_t)((t - S2_NT1) * 64 + kk[i]) * F_ + cb * 96 + nn[i];
        r[i] = *(const u32x4*)src;
      }
    };
    auto storeB = [&](int t, u32x4 (&r)[3]) {
      unsigned base = lds0 + (t % S2_NBUF) * S2_STAGE + 8192;
      LAS f32x2* st = (LAS f32x2*)(uintptr_t)(lds0 + S2_OFF_ST);
#pragma unroll
      for (int i = 0; i < 3; ++i) {
        u32x4 v = r[i];
        if (t >= S2_NT1) {
          f32x2 sc = st[(t - S2_NT1) * 64 + kk[i]];
#pragma unroll
          for (int q = 0; q < 4; ++q)
            v[q] = pack_bf16(__builtin_fmaf(bf_lo(v[q]), sc[0], sc[1]), __builtin_fmaf(bf_hi(v[q]), sc[0], sc[1]));
        }
        *(LAS u32x4*)(uintptr_t)(base + (i * 256 + pt) * 16) = v;
      }
    };
    u32x4 r0[3], r1[3], r2[3];
    loadB(0, r0);
    loadB(1, r1);
    loadB(2, r2);
    WAIT_LGKM0();
    BARRIER();
    storeB(0, r0);
    loadB(3, r0);
    WAIT_LGKM0();
    BARRIER();
#pragma unroll 1
    for (int t = 0; t < S2_NT; t += 3) {
      if (t + 1 < S2_NT) storeB(t + 1, r1);
      if (t + 4 < S2_NT) loadB(t + 4, r1);
      WAIT_LGKM0();
      BARRIER();
      if (t + 2 < S2_NT) storeB(t + 2, r2);
      if (t + 5 < S2_NT) loadB(t + 5, r2);
      WAIT_LGKM0();
      BARRIER();
      if (t + 3 < S2_NT) storeB(t + 3, r0);
      if (t + 6 < S2_NT) loadB(t + 6, r0);
      WAIT_LGKM0();
      BARRIER();
    }
  }
}

constexpr int S3_STAGE = 16384 + 20480;
constexpr int S3_NBUF = 4;
constexpr int S3_LDS = S3_NBUF * S3_STAGE;
constexpr int S3_NT = KA_ / 128;

__global__ __launch_bounds__(256, 2) void stage3_kernel(Params p) {
  extern __shared__ __attribute__((aligned(16))) char shm[];
  const unsigned lds0 = (unsigned)(uintptr_t)(LAS char*)shm;
  const int b = blockIdx.x, xcd = b & 7, j = b >> 3;
  const int rg = xcd * 4 + (j & 3), cb = j >> 2;
  const int tid = threadIdx.x, lane = tid & 63;
  const int wid = __builtin_amdgcn_readfirstlane(tid >> 6);
  const int fr = lane & 15, g = lane >> 4;
  constexpr int NB = 5;
  const u16* ua = (const u16*)(p.ws + WS_UA);
  const u16* M2 = (const u16*)(p.ws + WS_M2);
  const u16* asrc[4];
#pragma unroll
  for (int i = 0; i < 4; ++i) {
    int u = i * 4 + wid;
    int half = u >> 3, uu = u & 7;
    int row = uu * 8 + (lane >> 3), c = (lane & 7) ^ ((row >> 1) & 7);
    asrc[i] = ua + (size_t)(rg * 64 + row) * KA_ + half * 64 + c * 8;
  }
  unsigned boff[5];
#pragma unroll
  for (int i = 0; i < 5; ++i) {
    int u = i * 4 + wid, k, n;
    bslot_kn<NB>(u * 64 + lane, k, n);
    boff[i] = k * D_ + cb * 80 + n;
  }
  auto issue = [&](int t, int buf) {
    unsigned base = lds0 + buf * S3_STAGE;
    const u16* bm = t < 5 ? M2 + (size_t)t * 128 * D_ : p.w_out + (size_t)(t - 5) * 128 * D_;
#pragma unroll
    for (int i = 0; i < 4; ++i) glds16(asrc[i] + t * 128, base + (i * 4 + wid) * 1024);
#pragma unroll
    for (int i = 0; i < 5; ++i) glds16(bm + boff[i], base + 16384 + (i * 4 + wid) * 1024);
  };
  unsigned abase[2], bbase[2];
  {
    int x7 = (fr >> 1) & 7;
#pragma unroll
    for (int ks = 0; ks < 2; ++ks) abase[ks] = lds0 + (wid * 16 + fr) * 128 + (((ks * 4 + g) ^ x7) << 4);
    int q = fr >> 2, pp = fr & 3;
#pragma unroll
    for (int t = 0; t < 2; ++t) bbase[t] = lds0 + 16384 + (g * NB) * 256 + (((4 * t + q) ^ ((g & 1) << 2)) << 5) + pp * 8;
  }
  f32x4 acc[5];
#pragma unroll
  for (int n = 0; n < 5; ++n) acc[n] = f32x4{0, 0, 0, 0};
  issue(0, 0);
  issue(1, 1);
  issue(2, 2);
  int buf = 0;
#pragma unroll 1
  for (int t = 0; t < S3_NT; ++t) {
    if (t < S3_NT - 2) WAIT_VM(18);
    else if (t == S3_NT - 2) WAIT_VM(9);
    else WAIT_VM(0);
    WAIT_LGKM0();
    BARRIER();
    if (t + 3 < S3_NT) issue(t + 3, buf == 0 ? 3 : buf - 1);
    unsigned sb = buf * S3_STAGE;
#pragma unroll
    for (int k4 = 0; k4 < 4; ++k4) {
      bf16x8 af = lds_b128(abase[k4 & 1] + sb + (k4 >> 1) * 8192);
      bf16x8 bq[5];
#pragma unroll
      for (int n = 0; n < 5; ++n)
        bq[n] = lds_tr(bbase[0] + sb + k4 * 4 * NB * 256 + n * 256, bbase[1] + sb + k4 * 4 * NB * 256 + n * 256);
#pragma unroll
      for (int n = 0; n < 5; ++n) acc[n] = mfma16(bq[n], af, acc[n]);
    }
    buf = buf == S3_NBUF - 1 ? 0 : buf + 1;
  }
  int s = rg * 64 + wid * 16 + fr;
  const u16* xs = p.x + (size_t)s * D_ + cb * 80 + g * 4;
  u16* dst = p.out + (size_t)s * D_ + cb * 80 + g * 4;
#pragma unroll
  for (int n = 0; n < 5; ++n) {
    u32x2 xv = *(const u32x2*)(xs + n * 16);
    u32x2 w = {pack_bf16(bf_lo(xv[0]) + acc[n][0], bf_hi(xv[0]) + acc[n][1]),
               pack_bf16(bf_lo(xv[1]) + acc[n][2], bf_hi(xv[1]) + acc[n][3])};
    *(u32x2*)(dst + n * 16) = w;
  }
}

extern "C" void kernel_launch(void* const* d_in, const int* in_sizes, int n_in, void* d_out, int out_size, void* d_ws,
                              size_t ws_size, hipStream_t stream) {
  Params p;
  p.x = (const u16*)d_in[0];
  p.wqkv = (const u16*)d_in[1];
  p.wo = (const u16*)d_in[2];
  p.w_in = (const u16*)d_in[4];
  p.w_sp = (const u16*)d_in[5];
  p.b_sp = (const u16*)d_in[6];
  p.w_out = (const u16*)d_in[7];
  p.out = (u16*)d_out;
  p.ws = (char*)d_ws;
  (void)hipFuncSetAttribute((const void*)stage1_kernel, hipFuncAttributeMaxDynamicSharedMemorySize, S1_LDS);
  (void)hipFuncSetAttribute((const void*)stage2_kernel, hipFuncAttributeMaxDynamicSharedMemorySize, S2_LDS);
  (void)hipFuncSetAttribute((const void*)stage3_kernel, hipFuncAttributeMaxDynamicSharedMemorySize, S3_LDS);
  stage1_kernel<<<256, 256, S1_LDS, stream>>>(p);
  stage2_kernel<<<256, 512, S2_LDS, stream>>>(p);
  stage3_kernel<<<256, 256, S3_LDS, stream>>>(p);
}
```

```cpp
#if defined(__HIP_DEVICE_COMPILE__) && __HIP_DEVICE_COMPILE__
#pragma clang attribute push(__attribute__((target("no-packed-fp32-ops"))), apply_to = function)
#endif
#include <hip/hip_runtime.h>
#include <hip/hip_bf16.h>
#include <stdint.h>
#include <type_traits>

typedef short bf16x8 __attribute__((ext_vector_type(8)));
typedef short s16x4 __attribute__((ext_vector_type(4)));
typedef float f32x4 __attribute__((ext_vector_type(4)));
typedef float f32x2 __attribute__((ext_vector_type(2)));
typedef unsigned u32x4 __attribute__((ext_vector_type(4)));
typedef unsigned u32x2 __attribute__((ext_vector_type(2)));
typedef __bf16 bf16x2_t __attribute__((ext_vector_type(2)));
typedef unsigned short u16;

#define TIDX ((int)__builtin_amdgcn_workitem_id_x())
#define BIDX ((int)__builtin_amdgcn_workgroup_id_x())
#define LAS __attribute__((address_space(3)))
#define DI __device__ __forceinline__

constexpr int S_ = 2048, D_ = 640, H_ = 32, DH_ = 20, F_ = 768, KA_ = D_ + F_;

constexpr size_t WS_Y2 = 0;
constexpr size_t WS_Q = WS_Y2 + (size_t)S_ * F_ * 2;
constexpr size_t WS_UA = WS_Q + (size_t)S_ * D_ * 2;
constexpr size_t WS_B3 = WS_UA + (size_t)S_ * KA_ * 2;
constexpr size_t WS_KVP = WS_B3 + (size_t)KA_ * D_ * 2;
constexpr size_t WS_XST = WS_KVP + (size_t)16 * 32 * 420 * 4;
constexpr size_t WS_Z2N = WS_XST + (size_t)S_ * 2 * 4;
constexpr size_t WS_END = WS_Z2N + (size_t)S_ * F_ * 2;
constexpr size_t WS_FLAGS = WS_END;
constexpr size_t WS_KVR = WS_FLAGS + 64 * 32 * 4;

DI unsigned pack_bf16(float a, float b) {
  f32x2 v = {a, b};
  bf16x2_t r = __builtin_convertvector(v, bf16x2_t);
  return __builtin_bit_cast(unsigned, r);
}
DI float bf_lo(unsigned w) { return __builtin_bit_cast(float, w << 16); }
DI float bf_hi(unsigned w) { return __builtin_bit_cast(float, w & 0xffff0000u); }
DI float phi_f(float t) {
  return (fmaxf(t, 0.f) + 1e-6f) + __builtin_amdgcn_exp2f(fminf(t, 0.f) * 1.4426950408889634f);
}
DI float gelu_f(float x) {
  constexpr float c1 = -2.f * 0.7978845608028654f * 1.4426950408889634f;
  constexpr float c3 = c1 * 0.044715f;
  float a = x * __builtin_fmaf(x * x, c3, c1);
  return x * __builtin_amdgcn_rcpf(1.f + __builtin_amdgcn_exp2f(a));
}
DI float wave_sum(float v) {
  v += __builtin_bit_cast(float, __builtin_amdgcn_update_dpp(0, __builtin_bit_cast(int, v), 0xB1, 0xf, 0xf, false));
  v += __builtin_bit_cast(float, __builtin_amdgcn_update_dpp(0, __builtin_bit_cast(int, v), 0x4E, 0xf, 0xf, false));
  v += __builtin_bit_cast(float, __builtin_amdgcn_update_dpp(0, __builtin_bit_cast(int, v), 0x141, 0xf, 0xf, false));
  v += __builtin_bit_cast(float, __builtin_amdgcn_update_dpp(0, __builtin_bit_cast(int, v), 0x140, 0xf, 0xf, false));
  int iv = __builtin_bit_cast(int, v);
  float r0 = __builtin_bit_cast(float, __builtin_amdgcn_readlane(iv, 0)), r1 = __builtin_bit_cast(float, __builtin_amdgcn_readlane(iv, 16));
  float r2 = __builtin_bit_cast(float, __builtin_amdgcn_readlane(iv, 32)), r3 = __builtin_bit_cast(float, __builtin_amdgcn_readlane(iv, 48));
  return (r0 + r1) + (r2 + r3);
}
DI f32x4 mfma16(bf16x8 a, bf16x8 b, f32x4 c) { return __builtin_amdgcn_mfma_f32_16x16x32_bf16(a, b, c, 0, 0, 0); }
DI bf16x8 lds_b128(unsigned addr) { return *(LAS bf16x8*)(uintptr_t)addr; }
DI s16x4 lds_tr1(unsigned a) { return __builtin_amdgcn_ds_read_tr16_b64_v4i16((LAS s16x4*)(uintptr_t)a); }
DI bf16x8 cat8(s16x4 lo, s16x4 hi) { return __builtin_shufflevector(lo, hi, 0, 1, 2, 3, 4, 5, 6, 7); }
DI void glds16(const void* sbase, unsigned voff, unsigned lds_addr) {
  unsigned sv;
  asm volatile("s_mov_b32 %0, m0\n\ts_mov_b32 m0, %3\n\ts_nop 0\n\tglobal_load_lds_dwordx4 %1, %2\n\ts_mov_b32 m0, %0"
               : "=&s"(sv) : "v"(voff), "s"(sbase), "s"(lds_addr));
}
DI void touch_line(unsigned& sink, const void* sbase, unsigned voff) {
  asm volatile("global_load_dword %0, %1, %2" : "=v"(sink) : "v"(voff), "s"(sbase));
}
#define WAIT_VM(n) asm volatile("s_waitcnt vmcnt(%0)" ::"i"(n) : "memory")
#define WAIT_LGKM0() asm volatile("s_waitcnt lgkmcnt(0)" ::: "memory")
#define BARRIER() do { asm volatile("" ::: "memory"); __builtin_amdgcn_s_barrier(); asm volatile("" ::: "memory"); } while (0)
#define SBAR() __builtin_amdgcn_sched_barrier(0)
DI unsigned pinv(unsigned a) { asm volatile("" : "+v"(a)); return a; }

#define STAMP(kid, idx)

struct Params {
  const u16* x;
  const u16* wqkv;
  const u16* wo;
  const u16* w_in;
  const u16* w_sp;
  const u16* b_sp;
  const u16* w_out;
  u16* out;
  char* ws;
};

template <bool XQ>
DI int bswz(int k, int c) { return c ^ (((k >> 3) & 1) << 1) ^ (XQ ? (((k >> 1) & 1) << 2) : 0); }

constexpr int S1_STAGE = 16384 + 24576;
constexpr int S1_NBUF = 4;
constexpr int S1_LDS = S1_NBUF * S1_STAGE;
constexpr int S1_THREADS = 768;

template <int TYPE>
DI void stage1_body(const Params& p, int rg, int ct, unsigned lds0, int xcd, int jb) {
  constexpr int BN = TYPE == 2 ? 192 : 160;
  constexpr int CPR = BN / 8;
  constexpr int NF = BN / 32;
  constexpr int BI = BN / 32;
  constexpr int G = 4 + BI;
  constexpr int NT = D_ / 64;
  constexpr bool XQ = TYPE == 2;
  const int tid = TIDX, lane = tid & 63;
  const int wid = __builtin_amdgcn_readfirstlane(tid >> 6);
  const int fr = lane & 15, g = lane >> 4;
  const int wr = (wid >> 1) & 3, wc = wid & 1;

  LAS float* st_rs = (LAS float*)(uintptr_t)lds0;
  LAS float* st_sq = st_rs + 128;
  LAS float* st_cs = (LAS float*)(uintptr_t)(lds0 + 1024);
  const unsigned otile = lds0 + 4096;
  constexpr int OST = BN * 2 + 16;

  if (wid >= 8) {
    const int lw = wid - 8;
    const u16* Bm = TYPE == 2 ? p.w_in : p.wqkv;
    constexpr int ldb = TYPE == 2 ? 2 * F_ : 3 * D_;
    unsigned aoff, boff[BI];
    {
      int row = lw * 8 + (lane >> 3), c = (lane & 7) ^ ((row >> 1) & 7);
      aoff = ((rg * 128 + row) * D_ + c * 8) * 2;
    }
#pragma unroll
    for (int i = 0; i < BI; ++i) {
      int slot = (i * 4 + lw) * 64 + lane;
      int k = slot / CPR, c = bswz<XQ>(k, slot - k * CPR);
      int col;
      if (TYPE == 0) col = c < 10 ? (D_ + ct * 80 + c * 8) : (2 * D_ + ct * 80 + (c - 10) * 8);
      else if (TYPE == 1) col = ct * 160 + c * 8;
      else col = F_ + ct * 192 + c * 8;
      boff[i] = (k * ldb + col) * 2;
    }
    auto issue = [&](int t) {
      unsigned base = lds0 + (t & 3) * S1_STAGE;
#pragma unroll
      for (int i = 0; i < 4; ++i) glds16(p.x + t * 64 + i * 32 * D_, aoff, base + (i * 4 + lw) * 1024);
#pragma unroll
      for (int i = 0; i < BI; ++i) glds16(Bm + (size_t)t * 64 * ldb, boff[i], base + 16384 + (i * 4 + lw) * 1024);
    };
    issue(0);
    issue(1);
    unsigned sk[5];
    WAIT_VM(G);
    BARRIER();
    issue(2);
    WAIT_VM(G);
    BARRIER();
#pragma unroll 1
    for (int t = 0; t < NT - 4; ++t) {
      issue(t + 3);
      WAIT_VM(G);
      BARRIER();
    }
    issue(NT - 1);
    WAIT_VM(G);
    BARRIER();
    WAIT_VM(0);
    BARRIER();
    {
      const int lt = lw * 64 + lane;
      touch_line(sk[0], p.w_sp, (((xcd & 3) * 32 + jb) * 512 + lt) * 128);
      touch_line(sk[1], p.w_sp, (((xcd & 3) * 32 + jb) * 512 + 256 + lt) * 128);
      int i = lt < 120 ? lt : 0;
      touch_line(sk[2], p.w_in, ((jb * 20 + i / 6) * (2 * F_) + (xcd >> 2) * 384) * 2 + (i % 6) * 128);
      int i2 = (jb * 8 + xcd) * 30 + (lt < 30 ? lt : 0);
      int i3 = (jb * 8 + xcd) * 25 + (lt < 25 ? lt : 0);
      touch_line(sk[3], p.w_out, i2 * 128);
      touch_line(sk[4], p.wo, i3 * 128);
    }
    BARRIER();
    BARRIER();
    BARRIER();
    BARRIER();
    asm volatile("s_waitcnt vmcnt(0)" : "+v"(sk[0]), "+v"(sk[1]), "+v"(sk[2]), "+v"(sk[3]), "+v"(sk[4]) :: "memory");
    return;
  } else {
    unsigned sink;
    {
      const int kt = wid + 2, cq = xcd >> 1;
      const char* a;
      if (lane < 32) {
        a = (const char*)p.x + (size_t)((xcd & 1) * 1024 + jb * 32 + lane) * (D_ * 2) + kt * 128;
      } else {
        int i = jb * 30 + (lane < 62 ? lane - 32 : 0), row = kt * 64 + i / 15, sl = i % 15;
        a = sl < 12 ? (const char*)p.wqkv + (((row * (3 * D_) + (sl >> 2) * D_ + cq * 160) * 2 + ((sl & 3) < 3 ? (sl & 3) * 128 : 318)) & ~3)
                    : (const char*)p.w_in + (row * (2 * F_) + F_ + cq * 192) * 2 + (sl - 12) * 128;
      }
      asm volatile("global_load_dword %0, %1, off" : "=v"(sink) : "v"(a));
    }
    unsigned abase[2], baddr[NF];
    {
      int x7 = (fr >> 1) & 7;
#pragma unroll
      for (int ks = 0; ks < 2; ++ks) abase[ks] = lds0 + (wr * 32 + wc * 16 + fr) * 128 + (((ks * 4 + g) ^ x7) << 4);
      int q = fr >> 2, pp = fr & 3;
#pragma unroll
      for (int n = 0; n < NF; ++n) {
        int c;
        if (TYPE == 0) {
          int w = 2 * n + (pp >> 1);
          c = w < 5 ? wc * 5 + w : 10 + wc * 5 + (w - 5);
        } else {
          c = 2 * (wc * NF + n) + (pp >> 1);
        }
        int k = 8 * g + q;
        baddr[n] = lds0 + 16384 + (k * CPR + bswz<XQ>(k, c)) * 16 + (pp & 1) * 8;
      }
    }
    const int am1 = wc ? -2048 : 2048;
    f32x4 acc[2][NF], cs[NF], rs, sq;
#pragma unroll
    for (int m = 0; m < 2; ++m)
#pragma unroll
      for (int n = 0; n < NF; ++n) acc[m][n] = f32x4{0, 0, 0, 0};
#pragma unroll
    for (int n = 0; n < NF; ++n) cs[n] = f32x4{0, 0, 0, 0};
    rs = f32x4{0, 0, 0, 0};
    sq = f32x4{0, 0, 0, 0};
    const short one = 0x3f80;
    bf16x8 ones = {one, one, one, one, one, one, one, one};
    asm volatile("" : "+v"(ones));

    bf16x8 af0[2], af1[2];
    s16x4 bl0[NF], bh0[NF], bl1[NF], bh1[NF];
    auto phase = [&](bf16x8 (&afc)[2], s16x4 (&blc)[NF], s16x4 (&bhc)[NF], bf16x8 (&afn)[2], s16x4 (&bln)[NF], s16x4 (&bhn)[NF],
                     unsigned sbn, int ksn, bool docs) {
      const unsigned a0 = pinv(abase[ksn] + sbn);
      unsigned ba = 0;
#pragma unroll
      for (int i = 0; i < 2 * NF + 2; ++i) {
        if (i < 2 * NF) {
          int m = i / NF, n = i % NF;
          bf16x8 bq = cat8(blc[n], bhc[n]);
          acc[m][n] = TYPE == 0 ? mfma16(afc[m], bq, acc[m][n]) : mfma16(bq, afc[m], acc[m][n]);
        } else if (i == 2 * NF) rs = mfma16(afc[0], ones, rs);
        else sq = mfma16(afc[0], afc[0], sq);
        if (i == 0) afn[0] = lds_b128(a0);
        else if (i <= 2 * NF) {
          int r = i - 1;
          if (r & 1) bhn[r >> 1] = lds_tr1(ba + (ksn * 32 + 4) * CPR * 16);
          else { ba = pinv(baddr[r >> 1] + sbn); bln[r >> 1] = lds_tr1(ba + ksn * 32 * CPR * 16); }
        } else afn[1] = lds_b128(a0 + am1);
        SBAR();
      }
      if (docs) {
#pragma unroll
        for (int n = 0; n < NF; ++n) cs[n] = mfma16(ones, cat8(blc[n], bhc[n]), cs[n]);
      }
      SBAR();
    };
    STAMP(0, 0);
    BARRIER();
    af0[0] = lds_b128(abase[0]);
    af0[1] = lds_b128(abase[0] + am1);
#pragma unroll
    for (int n = 0; n < NF; ++n) { bl0[n] = lds_tr1(baddr[n]); bh0[n] = lds_tr1(baddr[n] + 4 * CPR * 16); }
    phase(af0, bl0, bh0, af1, bl1, bh1, 0, 1, wr == 0);
    BARRIER();
    phase(af1, bl1, bh1, af0, bl0, bh0, S1_STAGE, 0, wr == 1);
    BARRIER();
#pragma unroll 1
    for (int t = 1; t < NT; ++t) {
      STAMP(0, 1 + t);
      phase(af0, bl0, bh0, af1, bl1, bh1, (t & 3) * S1_STAGE, 1, wr == ((2 * t) & 3));
      phase(af1, bl1, bh1, af0, bl0, bh0, ((t + 1) & 3) * S1_STAGE, 0, wr == ((2 * t + 1) & 3));
      if (t == NT - 1) WAIT_LGKM0();
      BARRIER();
    }
    STAMP(0, 100);
    asm volatile("s_waitcnt vmcnt(0)" : "+v"(sink) :: "memory");
    {
      int r0 = wr * 32 + wc * 16;
      if (fr == 0) *(LAS f32x4*)(st_rs + r0 + g * 4) = rs;
      if ((fr >> 2) == g) {
        int r = fr & 3;
        st_sq[r0 + fr] = r == 0 ? sq[0] : r == 1 ? sq[1] : r == 2 ? sq[2] : sq[3];
      }
      if (g == 0) {
#pragma unroll
        for (int n = 0; n < NF; ++n) st_cs[wr * 192 + wc * NF * 16 + n * 16 + fr] = cs[n][0];
      }
    }
    WAIT_LGKM0();
    BARRIER();
    STAMP(0, 102);
    if (TYPE == 0) {
      float csv[NF];
#pragma unroll
      for (int n = 0; n < NF; ++n) {
        int c0 = wc * NF * 16 + n * 16 + fr;
        csv[n] = (st_cs[c0] + st_cs[192 + c0]) + (st_cs[384 + c0] + st_cs[576 + c0]);
      }
      bf16x8 xf[NF];
#pragma unroll
      for (int m = 0; m < 2; ++m) {
        int r0 = wr * 32 + (m ^ wc) * 16 + g * 4;
        f32x4 mean = *(LAS f32x4*)(st_rs + r0) * (1.f / D_);
        f32x4 ex2 = *(LAS f32x4*)(st_sq + r0) * (1.f / D_);
        f32x4 rstd, nm;
#pragma unroll
        for (int r = 0; r < 4; ++r) {
          rstd[r] = __builtin_amdgcn_rsqf(fmaxf(ex2[r] - mean[r] * mean[r], 0.f) + 1e-5f);
          nm[r] = -mean[r] * rstd[r];
        }
#pragma unroll
        for (int n = 0; n < NF; ++n) {
          f32x4 y = acc[m][n] * rstd + nm * csv[n];
          bool isk = (n * 16 + fr) < 40;
          float v[4];
#pragma unroll
          for (int r = 0; r < 4; ++r) v[r] = n < 2 ? phi_f(y[r]) : (n == 2 ? (isk ? phi_f(y[r]) : y[r]) : y[r]);
          u32x4 t4 = __builtin_bit_cast(u32x4, xf[n]);
          t4[m * 2] = pack_bf16(v[0], v[1]);
          t4[m * 2 + 1] = pack_bf16(v[2], v[3]);
          xf[n] = __builtin_bit_cast(bf16x8, t4);
        }
      }
      SBAR();
      STAMP(0, 104);
      constexpr int PI[7] = {0, 0, 1, 1, 1, 2, 2};
      constexpr int PJ[7] = {2, 3, 2, 3, 4, 3, 4};
      f32x4 dd[10];
#pragma unroll
      for (int q = 0; q < 7; ++q) dd[q] = mfma16(xf[PI[q]], xf[PJ[q]], f32x4{0, 0, 0, 0});
#pragma unroll
      for (int i = 0; i < 3; ++i) dd[7 + i] = mfma16(xf[i], ones, f32x4{0, 0, 0, 0});
      SBAR();
#pragma unroll
      for (int q = 0; q < 10; ++q) *(LAS f32x4*)(uintptr_t)(otile + wid * 10240 + q * 1024 + lane * 16) = dd[q];
    } else {
      f32x4 csv[NF];
#pragma unroll
      for (int n = 0; n < NF; ++n) {
        int c0 = wc * NF * 16 + n * 16 + g * 4;
        csv[n] = (*(LAS f32x4*)(st_cs + c0) + *(LAS f32x4*)(st_cs + 192 + c0)) + (*(LAS f32x4*)(st_cs + 384 + c0) + *(LAS f32x4*)(st_cs + 576 + c0));
      }
#pragma unroll
      for (int m = 0; m < 2; ++m) {
        int rl = wr * 32 + (m ^ wc) * 16 + fr;
        float mean = st_rs[rl] * (1.f / D_);
        float ex2 = st_sq[rl] * (1.f / D_);
        float rstd = __builtin_amdgcn_rsqf(fmaxf(ex2 - mean * mean, 0.f) + 1e-5f);
        float nm = -mean * rstd;
        if (TYPE == 2 && ct == 0 && m == 0 && g == 0) *(f32x2*)((float*)(p.ws + WS_XST) + (size_t)(rg * 128 + rl) * 2) = f32x2{nm, rstd};
        unsigned dst = otile + rl * OST + (wc * NF * 16 + g * 4) * 2;
#pragma unroll
        for (int n = 0; n < NF; ++n) {
          f32x4 y = acc[m][n] * rstd + csv[n] * nm;
          *(LAS u32x2*)(uintptr_t)(dst + n * 32) = u32x2{pack_bf16(y[0], y[1]), pack_bf16(y[2], y[3])};
        }
      }
    }
    STAMP(0, 105);
  }
  WAIT_LGKM0();
  BARRIER();
  STAMP(0, 106);
  if (TYPE == 0) {
    float* kvp = (float*)(p.ws + WS_KVP) + ((size_t)rg * 32 + ct * 4) * 420;
    for (int i = tid; i < 1680; i += 512) {
      int hl = i / 420, w = i - hl * 420;
      int hi = hl & 1, ci, cj, q;
      if (w < 400) {
        int d = w / 20, e = w - d * 20;
        ci = hi * 20 + d; cj = 40 + hi * 20 + e;
        int pi = ci >> 4, pj = cj >> 4;
        q = pi == 0 ? pj - 2 : (pi == 1 ? pj : pj + 2);
      } else {
        ci = hi * 20 + (w - 400); cj = 0;
        q = 7 + (ci >> 4);
      }
      unsigned a = otile + (hl >> 1) * 10240 + q * 1024 + ((((ci & 15) >> 2) * 16 + (cj & 15)) * 4 + (ci & 3)) * 4;
      float v = (*(LAS float*)(uintptr_t)(a) + *(LAS float*)(uintptr_t)(a + 2 * 10240)) +
                (*(LAS float*)(uintptr_t)(a + 4 * 10240) + *(LAS float*)(uintptr_t)(a + 6 * 10240));
      kvp[i] = v;
    }
  } else {
    u16* dstm = TYPE == 1 ? (u16*)(p.ws + WS_Q) + (size_t)rg * 128 * D_ + ct * 160
                          : (u16*)(p.ws + WS_Y2) + (size_t)rg * 128 * F_ + ct * 192;
    constexpr int ldo = TYPE == 1 ? D_ : F_;
    for (int idx = tid; idx < 128 * CPR; idx += 512) {
      int row = idx / CPR, c = idx - row * CPR;
      u32x4 v = *(LAS u32x4*)(uintptr_t)(otile + row * OST + c * 16);
      *(u32x4*)(dstm + (size_t)row * ldo + c * 8) = v;
    }
  }
}

__global__ __launch_bounds__(S1_THREADS, 3) void stage1_kernel(Params p) {
  extern __shared__ __attribute__((aligned(16))) char shm[];
  unsigned lds0 = (unsigned)(uintptr_t)(LAS char*)shm;
  int b = BIDX, xcd = b & 7, j = b >> 3;
  int rg = (xcd & 1) * 8 + (j & 7);
  int cq = xcd >> 1, ci = j >> 3;
  STAMP(0, 200);
  if (b == 0) {
    for (int i = TIDX; i < 64 * 32; i += S1_THREADS) ((unsigned*)(p.ws + WS_FLAGS))[i] = 0u;
  }
  if (ci < 2) stage1_body<0>(p, rg, cq * 2 + ci, lds0, xcd, j);
  else if (ci == 2) stage1_body<1>(p, rg, cq, lds0, xcd, j);
  else stage1_body<2>(p, rg, cq, lds0, xcd, j);
  STAMP(0, 201);
}

constexpr int K2_SLOT = 8192 + 12288;
constexpr int K2_NS = 7;
constexpr int K2_SCR = K2_NS * K2_SLOT;
constexpr int K2_LDS = K2_SCR + 20480;
constexpr int K2_MID = (K2_NS - 1) * K2_SLOT;
constexpr int K2_THREADS = 1024;
constexpr int T_G = 10, T_A = 42, T_B = 52, T_END = 64;
constexpr unsigned SPIN_CAP = 1u << 22;

typedef __attribute__((address_space(1))) unsigned gu32;
DI void* uni_ptr(const void* q) {
  unsigned long long a = (unsigned long long)q;
  unsigned lo = __builtin_amdgcn_readfirstlane((unsigned)a), hi = __builtin_amdgcn_readfirstlane((unsigned)(a >> 32));
  return (void*)(((unsigned long long)hi << 32) | lo);
}
DI void store16_sc1(void* sbase, unsigned voff, u32x4 v) {
  sbase = uni_ptr(sbase);
  asm volatile("s_nop 4\n\tglobal_store_dwordx4 %0, %1, %2 sc1\n\ts_nop 1" ::"v"(voff), "v"(v), "s"(sbase) : "memory");
}
DI void store8_sc1(void* sbase, unsigned voff, u32x2 v) {
  asm volatile("global_store_dwordx2 %0, %1, %2 sc1\n\ts_nop 1" ::"v"(voff), "v"(v), "s"(sbase) : "memory");
}
DI void glds16_sc1(const void* sbase, unsigned voff, unsigned lds_addr) {
  unsigned sv;
  asm volatile("s_mov_b32 %0, m0\n\ts_mov_b32 m0, %3\n\ts_nop 0\n\tglobal_load_lds_dwordx4 %1, %2 sc1\n\ts_mov_b32 m0, %0"
               : "=&s"(sv) : "v"(voff), "s"(sbase), "s"(lds_addr));
}
DI void wait_flags(unsigned* flags, int nw, unsigned target, int lane) {
  gu32* fp = (gu32*)(flags + (lane < nw ? lane : 0) * 32);
  for (unsigned spins = 0; spins < SPIN_CAP; ++spins) {
    unsigned v = __hip_atomic_load(fp, __ATOMIC_RELAXED, __HIP_MEMORY_SCOPE_AGENT);
    if (__all(v >= target)) break;
    __builtin_amdgcn_s_sleep(1);
  }
  __builtin_amdgcn_fence(__ATOMIC_ACQUIRE, "wavefront");
}

__global__ __launch_bounds__(K2_THREADS, 4) void stage2_kernel(Params p) {
  extern __shared__ __attribute__((aligned(16))) char shm[];
  const unsigned lds0 = (unsigned)(uintptr_t)(LAS char*)shm;
  const int b = BIDX, xcd = b & 7, j = b >> 3;
  const int rg = (xcd & 3) * 8 + (j & 7);
  const int cb = (xcd >> 2) * 4 + (j >> 3);
  const int rg3 = xcd * 4 + (j & 3), cb3 = j >> 2;
  const int tid = TIDX, lane = tid & 63;
  const int wid = __builtin_amdgcn_readfirstlane(tid >> 6);
  const int fr = lane & 15, g = lane >> 4;
  unsigned* flags = (unsigned*)(p.ws + WS_FLAGS);
  constexpr int CPR = 12;
  STAMP(1, 200);

  if (wid >= 12) {
    const int st = tid - 768, sw = wid - 12;
    const int mcb = b & 7;
    const unsigned scr = lds0 + K2_SCR;
    LAS float* zt = (LAS float*)(uintptr_t)(scr + 18432);
#define SB(n) do { _Pragma("unroll 1") for (int i_ = 0; i_ < (n); ++i_) BARRIER(); } while (0)
#define FLAG_ADD_S(off) do { if (st == 0) __hip_atomic_fetch_add((gu32*)(flags + (off)), 1u, __ATOMIC_RELAXED, __HIP_MEMORY_SCOPE_AGENT); } while (0)
    u32x4 w0, w1;
    u32x2 qw[5];
    {
      const u16* wsrc = b < 128 ? p.wo + (size_t)(b >> 3) * 40 * D_ : p.w_out + (size_t)((b - 128) >> 3) * 48 * D_;
      const int nch = b < 128 ? 400 : 480;
      int i1 = st + 256 < nch ? st + 256 : 0;
      w0 = *(const u32x4*)(wsrc + (st / 10) * D_ + mcb * 80 + (st % 10) * 8);
      w1 = *(const u32x4*)(wsrc + (i1 / 10) * D_ + mcb * 80 + (i1 % 10) * 8);
      const u16* qsrc = (const u16*)(p.ws + WS_Q) + (size_t)((b >> 1) * 16 + (st >> 4)) * D_ + (b & 1) * 320 + (st & 15) * 20;
#pragma unroll
      for (int q = 0; q < 5; ++q) qw[q] = *(const u32x2*)(qsrc + q * 4);
    }
    SB(3);
    float pq[20];
#pragma unroll
    for (int q = 0; q < 3; ++q) {
      pq[q * 4] = phi_f(bf_lo(qw[q][0])); pq[q * 4 + 1] = phi_f(bf_hi(qw[q][0]));
      pq[q * 4 + 2] = phi_f(bf_lo(qw[q][1])); pq[q * 4 + 3] = phi_f(bf_hi(qw[q][1]));
    }
    SBAR();
    SB(1);
#pragma unroll
    for (int q = 3; q < 5; ++q) {
      pq[q * 4] = phi_f(bf_lo(qw[q][0])); pq[q * 4 + 1] = phi_f(bf_hi(qw[q][0]));
      pq[q * 4 + 2] = phi_f(bf_lo(qw[q][1])); pq[q * 4 + 3] = phi_f(bf_hi(qw[q][1]));
    }
    *(LAS u32x4*)(uintptr_t)(scr + 8192 + st * 16) = w0;
    if (st + 256 < (b < 128 ? 400 : 480)) *(LAS u32x4*)(uintptr_t)(scr + 8192 + (st + 256) * 16) = w1;
    if (b < 128) {
      *(LAS u32x4*)(uintptr_t)(scr + st * 16) = u32x4{0, 0, 0, 0};
      *(LAS u32x4*)(uintptr_t)(scr + 4096 + st * 16) = u32x4{0, 0, 0, 0};
      if (st < 240) *(LAS u32x4*)(uintptr_t)(scr + 8192 + 6400 + st * 16) = u32x4{0, 0, 0, 0};
    }
    WAIT_LGKM0();
    SB(3);
    if (sw == 0) wait_flags(flags, 16, 16u, lane);
    SB(1);
    u32x4 tb0, tb1;
    {
      unsigned o0 = (((b < 128 ? b >> 3 : 0) * 2 + (st < 200 ? st / 100 : 0)) * 420 + (st % 100) * 4) * 4;
      int i4 = (st < 80 ? st : 0) * 4;
      unsigned o1 = (((b & 1) * 16 + i4 / 20) * 420 + 400 + i4 % 20) * 4;
      asm volatile("global_load_dwordx4 %0, %1, %2 sc1" : "=v"(tb0) : "v"(o0), "s"(p.ws + WS_KVR));
      asm volatile("global_load_dwordx4 %0, %1, %2 sc1" : "=v"(tb1) : "v"(o1), "s"(p.ws + WS_KVR));
    }
    const int q4 = fr >> 2, pp = fr & 3;
    if (b >= 128) {
#pragma unroll
      for (int it = 0; it < 2; ++it) {
        int blk = (it * 4 + sw) * 4 + g;
        int bb = blk < 30 ? blk : 0;
        int kc = bb / 5, nf = bb - kc * 5;
        unsigned wa = scr + 8192 + (kc * 8 + q4) * 160 + nf * 32 + pp * 8;
        bf16x8 v = cat8(lds_tr1(wa), lds_tr1(wa + 4 * 160));
        if (blk < 30) store16_sc1(p.ws + WS_B3, ((mcb * 176 + 80 + ((b - 128) >> 3) * 6 + kc) * 80 + nf * 16 + fr) * 16, __builtin_bit_cast(u32x4, v));
      }
    }
    SB(5);
    asm volatile("s_waitcnt vmcnt(0)" : "+v"(tb0), "+v"(tb1) :: "memory");
    if (st < 80) *(LAS u32x4*)(zt + st * 4) = tb1;
    if (b < 128 && st < 200) {
      int hh = st / 100, w = (st - hh * 100) * 4, dl = w / 20, el = w - dl * 20;
      int d = hh * 20 + dl, e = hh * 20 + el;
      int row = (d >> 5) * 32 + ((d >> 2) & 1) * 16 + ((d >> 3) & 3) * 4 + (d & 3);
      f32x4 kf = __builtin_bit_cast(f32x4, tb0);
      unsigned a = scr + row * 128 + ((((e >> 3) ^ ((row >> 1) & 7)) << 4) | ((e & 4) << 1));
      *(LAS u32x2*)(uintptr_t)a = u32x2{pack_bf16(kf[0], kf[1]), pack_bf16(kf[2], kf[3])};
    }
    WAIT_LGKM0();
    SB(1);
    {
      float den = 0.f;
#pragma unroll
      for (int q = 0; q < 5; ++q) {
        f32x4 zz = *(LAS f32x4*)(zt + (st & 15) * 20 + q * 4);
        den += pq[q * 4] * zz[0] + pq[q * 4 + 1] * zz[1] + pq[q * 4 + 2] * zz[2] + pq[q * 4 + 3] * zz[3];
      }
      float inv = 1.f / (den + 1e-6f);
#pragma unroll
      for (int q = 0; q < 5; ++q)
        qw[q] = u32x2{pack_bf16(pq[q * 4] * inv, pq[q * 4 + 1] * inv), pack_bf16(pq[q * 4 + 2] * inv, pq[q * 4 + 3] * inv)};
    }
    SBAR();
    SB(1);
#pragma unroll
    for (int it = 0; it < 3; ++it) {
      if (b < 128) {
        int unit = sw + it * 4;
        int u = unit < 10 ? unit : 0;
        int db = u / 5, nf = u - db * 5;
        f32x4 d0 = {0, 0, 0, 0}, d1 = {0, 0, 0, 0};
#pragma unroll
        for (int ks = 0; ks < 2; ++ks) {
          unsigned wa = scr + 8192 + (ks * 32 + g * 8 + q4) * 160 + nf * 32 + pp * 8;
          bf16x8 bw = cat8(lds_tr1(wa), lds_tr1(wa + 4 * 160));
          unsigned aa = scr + (db * 32 + fr) * 128 + (((ks * 4 + g) ^ ((fr >> 1) & 7)) << 4);
          d0 = mfma16(lds_b128(aa), bw, d0);
          d1 = mfma16(lds_b128(aa + 2048), bw, d1);
        }
        int kc = db * 4 + g;
        if (unit < 10 && kc < 5) {
          u32x4 o = {pack_bf16(d0[0], d0[1]), pack_bf16(d0[2], d0[3]), pack_bf16(d1[0], d1[1]), pack_bf16(d1[2], d1[3])};
          store16_sc1(p.ws + WS_B3, ((mcb * 176 + (b >> 3) * 5 + kc) * 80 + nf * 16 + fr) * 16, o);
        }
      }
      WAIT_LGKM0();
      SB(1);
    }
    constexpr int PST = 640 + 16;
    {
      unsigned a = scr + (st >> 4) * PST + (st & 15) * 40;
#pragma unroll
      for (int q = 0; q < 5; ++q) *(LAS u32x2*)(uintptr_t)(a + q * 8) = qw[q];
    }
    WAIT_LGKM0();
    SB(1);
    {
      const int prg = b >> 3, r0 = ((b >> 1) & 3) * 16;
#pragma unroll
      for (int it = 0; it < 3; ++it) {
        int idx = st + it * 256;
        if (idx < 640) {
          int ktl = idx >> 7, row = (idx >> 3) & 15, cp = idx & 7;
          int c = cp ^ (((r0 + row) >> 1) & 7);
          u32x4 v = *(LAS u32x4*)(uintptr_t)(scr + row * PST + (ktl * 8 + c) * 16);
          store16_sc1(p.ws + WS_UA, ((prg * 22 + (b & 1) * 5 + ktl) * 64 + r0 + row) * 128 + cp * 16, v);
        }
      }
    }
    SB(5);
    WAIT_VM(0);
    SB(1);
    FLAG_ADD_S(512 + (b & 15) * 32);
    SB(12);
    if (sw == 0) wait_flags(flags + 512, 16, 16u, lane);
    SB(7);
    {
      constexpr int OST = 96 * 2 + 16;
#pragma unroll
      for (int it = 0; it < 3; ++it) {
        int idx = st + it * 256;
        int row = idx / CPR, c = idx - row * CPR;
        u32x4 v = *(LAS u32x4*)(uintptr_t)(scr + row * OST + c * 16);
        int cg = cb * 12 + c;
        store16_sc1(p.ws + WS_UA, ((rg * 22 + 10 + (cg >> 3)) * 64 + row) * 128 + (((cg & 7) ^ ((row >> 1) & 7)) << 4), v);
      }
    }
    SB(2);
    WAIT_VM(0);
    SB(1);
    FLAG_ADD_S(1024 + rg * 32);
    SB(6);
    if (sw == 0) wait_flags(flags + 1024 + rg3 * 32, 1, 8u, lane);
    SB(1);
  } else if (wid >= 8) {
    const int lw = wid - 8, lt = tid - 512;
    unsigned aoff1, aoff2, boff1[3];
    {
      int row = lw * 8 + (lane >> 3), c = (lane & 7) ^ ((row >> 1) & 7);
      aoff1 = ((rg * 64 + row) * D_ + c * 8) * 2;
      aoff2 = ((rg * 64 + row) * S_ + c * 8) * 2;
    }
#pragma unroll
    for (int i = 0; i < 3; ++i) {
      int slot = i * 256 + lt;
      int k = slot / CPR;
      boff1[i] = (k * (2 * F_) + cb * 96 + bswz<false>(k, slot - k * CPR) * 8) * 2;
    }
    const unsigned boff2 = lt * 16;
    const u16* zpanel = (const u16*)(p.ws + WS_Z2N) + (size_t)cb * S_ * 96;
    const u16* bpanel = (const u16*)(p.ws + WS_B3) + (size_t)cb3 * KA_ * 80;
    const u16* ua = (const u16*)(p.ws + WS_UA) + (size_t)rg3 * 22 * 4096;
    const bool b3 = lw < 2;
    const unsigned lbase = lds0 + lw * 1024;
    auto ld_z1_a = [&](int t, int sl) {
#pragma unroll
      for (int i = 0; i < 2; ++i) glds16(p.x + t * 64 + i * 32 * D_, aoff1, lbase + sl * K2_SLOT + i * 4096);
    };
    auto ld_z1_b = [&](int t, int sl) {
#pragma unroll
      for (int i = 0; i < 3; ++i) glds16(p.w_in + (size_t)t * 64 * (2 * F_), boff1[i], lbase + sl * K2_SLOT + 8192 + i * 4096);
    };
    auto ld_g_a = [&](int t, int sl) {
#pragma unroll
      for (int i = 0; i < 2; ++i) glds16(p.w_sp + t * 64 + i * 32 * S_, aoff2, lbase + sl * K2_SLOT + i * 4096);
    };
    auto ld_g_b = [&](int t, int sl) {
#pragma unroll
      for (int i = 0; i < 3; ++i) glds16_sc1(zpanel + (size_t)t * 64 * 96 + i * 2048, boff2, lbase + sl * K2_SLOT + 8192 + i * 4096);
    };
    auto ld_o_a = [&](int t, int sl) {
#pragma unroll
      for (int i = 0; i < 2; ++i) glds16_sc1(ua + t * 4096 + i * 2048, boff2, lbase + sl * K2_SLOT + i * 4096);
    };
    auto ld_o_b = [&](int t, int sl) {
#pragma unroll
      for (int i = 0; i < 2; ++i) glds16_sc1(bpanel + (size_t)t * 64 * 80 + i * 2048, boff2, lbase + sl * K2_SLOT + 8192 + i * 4096);
      if (b3) glds16_sc1(bpanel + (size_t)t * 64 * 80 + 4096, boff2, lbase + sl * K2_SLOT + 8192 + 8192);
    };
    ld_z1_a(0, 0); ld_z1_b(0, 0); ld_z1_a(1, 1); ld_z1_b(1, 1); WAIT_VM(5); BARRIER();
    ld_z1_a(2, 2); ld_z1_b(2, 2); ld_z1_a(3, 3); ld_z1_b(3, 3); WAIT_VM(10); BARRIER();
    ld_z1_a(4, 4); ld_z1_b(4, 4); ld_z1_a(5, 5); ld_z1_b(5, 5); ld_z1_a(6, 6); ld_z1_b(6, 6); WAIT_VM(20); BARRIER();
    ld_z1_a(7, 0); ld_z1_b(7, 0); WAIT_VM(20); BARRIER();
    ld_z1_a(8, 1); ld_z1_b(8, 1); WAIT_VM(20); BARRIER();
    ld_z1_a(9, 2); ld_z1_b(9, 2); WAIT_VM(20); BARRIER();
    ld_g_a(0, 3); WAIT_VM(17); BARRIER();
    ld_g_a(1, 4); WAIT_VM(14); BARRIER();
    ld_g_b(0, 3); ld_g_b(1, 4); ld_g_a(2, 5); ld_g_b(2, 5); WAIT_VM(20); BARRIER();
    ld_g_a(3, 6); ld_g_b(3, 6); WAIT_VM(20); BARRIER();
    ld_g_a(4, 0); ld_g_b(4, 0); WAIT_VM(18); BARRIER();
    ld_g_a(5, 1); ld_g_b(5, 1); WAIT_VM(20); BARRIER();
    ld_g_a(6, 2); ld_g_b(6, 2); WAIT_VM(20); BARRIER();
    ld_g_a(7, 3); ld_g_b(7, 3); WAIT_VM(20); BARRIER();
    ld_g_a(8, 4); ld_g_b(8, 4); WAIT_VM(20); BARRIER();
    ld_g_a(9, 5); ld_g_b(9, 5); WAIT_VM(20); BARRIER();
    ld_g_a(10, 6); ld_g_b(10, 6); WAIT_VM(20); BARRIER();
    ld_g_a(11, 0); ld_g_b(11, 0); WAIT_VM(20); BARRIER();
    ld_g_a(12, 1); ld_g_b(12, 1); WAIT_VM(20); BARRIER();
    ld_g_a(13, 2); ld_g_b(13, 2); WAIT_VM(20); BARRIER();
    ld_g_a(14, 3); ld_g_b(14, 3); WAIT_VM(20); BARRIER();
    ld_g_a(15, 4); ld_g_b(15, 4); WAIT_VM(20); BARRIER();
    ld_g_a(16, 5); ld_g_b(16, 5); WAIT_VM(20); BARRIER();
    ld_g_a(17, 6); ld_g_b(17, 6); WAIT_VM(20); BARRIER();
    ld_g_a(18, 0); ld_g_b(18, 0); WAIT_VM(20); BARRIER();
    ld_g_a(19, 1); ld_g_b(19, 1); WAIT_VM(20); BARRIER();
    ld_g_a(20, 2); ld_g_b(20, 2); WAIT_VM(20); BARRIER();
    ld_g_a(21, 3); ld_g_b(21, 3); WAIT_VM(20); BARRIER();
    ld_g_a(22, 4); ld_g_b(22, 4); WAIT_VM(20); BARRIER();
    ld_g_a(23, 5); ld_g_b(23, 5); WAIT_VM(20); BARRIER();
    ld_g_a(24, 6); ld_g_b(24, 6); WAIT_VM(20); BARRIER();
    ld_g_a(25, 0); ld_g_b(25, 0); WAIT_VM(20); BARRIER();
    ld_g_a(26, 1); ld_g_b(26, 1); WAIT_VM(20); BARRIER();
    ld_g_a(27, 2); ld_g_b(27, 2); WAIT_VM(20); BARRIER();
    ld_g_a(28, 3); ld_g_b(28, 3); WAIT_VM(20); BARRIER();
    ld_g_a(29, 4); ld_g_b(29, 4); WAIT_VM(20); BARRIER();
    ld_g_a(30, 5); ld_g_b(30, 5); WAIT_VM(20); BARRIER();
    ld_g_a(31, 6); ld_g_b(31, 6); WAIT_VM(20); BARRIER();
    ld_o_a(0, 0); ld_o_b(0, 0); if (b3) WAIT_VM(20); else WAIT_VM(19); BARRIER();
    ld_o_a(1, 1); ld_o_b(1, 1); if (b3) WAIT_VM(20); else WAIT_VM(18); BARRIER();
    ld_o_a(2, 2); ld_o_b(2, 2); if (b3) WAIT_VM(20); else WAIT_VM(17); BARRIER();
    ld_o_a(3, 3); ld_o_b(3, 3); if (b3) WAIT_VM(20); else WAIT_VM(16); BARRIER();
    ld_o_a(4, 4); ld_o_b(4, 4); if (b3) WAIT_VM(20); else WAIT_VM(16); BARRIER();
    ld_o_a(5, 5); ld_o_b(5, 5); if (b3) WAIT_VM(20); else WAIT_VM(16); BARRIER();
    ld_o_a(6, 6); ld_o_b(6, 6); if (b3) WAIT_VM(20); else WAIT_VM(16); BARRIER();
    ld_o_a(7, 0); ld_o_b(7, 0); if (b3) WAIT_VM(20); else WAIT_VM(16); BARRIER();
    ld_o_a(8, 1); ld_o_b(8, 1); if (b3) WAIT_VM(20); else WAIT_VM(16); BARRIER();
    ld_o_a(9, 2); ld_o_b(9, 2); if (b3) WAIT_VM(20); else WAIT_VM(16); BARRIER();
    ld_o_b(10, 3); if (b3) WAIT_VM(18); else WAIT_VM(14); BARRIER();
    ld_o_b(11, 4); if (b3) WAIT_VM(16); else WAIT_VM(12); BARRIER();
    ld_o_b(12, 5); if (b3) WAIT_VM(14); else WAIT_VM(10); BARRIER();
    ld_o_b(13, 6); if (b3) WAIT_VM(12); else WAIT_VM(8); BARRIER();
    ld_o_b(14, 0); BARRIER();
    ld_o_b(15, 1); BARRIER();
    ld_o_a(10, 3); ld_o_a(11, 4); ld_o_a(12, 5); ld_o_a(13, 6); ld_o_a(14, 0); ld_o_a(15, 1); WAIT_VM(10); BARRIER();
    WAIT_VM(8); BARRIER();
    ld_o_a(16, 2); ld_o_b(16, 2); if (b3) WAIT_VM(11); else WAIT_VM(10); BARRIER();
    ld_o_a(17, 3); ld_o_b(17, 3); if (b3) WAIT_VM(14); else WAIT_VM(12); BARRIER();
    ld_o_a(18, 4); ld_o_b(18, 4); if (b3) WAIT_VM(17); else WAIT_VM(14); BARRIER();
    ld_o_a(19, 5); ld_o_b(19, 5); if (b3) WAIT_VM(20); else WAIT_VM(16); BARRIER();
    ld_o_a(20, 6); ld_o_b(20, 6); if (b3) WAIT_VM(20); else WAIT_VM(16); BARRIER();
    ld_o_a(21, 0); ld_o_b(21, 0); if (b3) WAIT_VM(20); else WAIT_VM(16); BARRIER();
    if (b3) WAIT_VM(15); else WAIT_VM(12); BARRIER();
    if (b3) WAIT_VM(10); else WAIT_VM(8); BARRIER();
    if (b3) WAIT_VM(5); else WAIT_VM(4); BARRIER();
    WAIT_VM(0); BARRIER();
    BARRIER();
    BARRIER();
  } else {
    const int wr = wid >> 1, wc = wid & 1;
    const int kh = wid >> 2, wm = wid & 3;
    const int mcb = b & 7;
    const unsigned scr = lds0 + K2_SCR;
    LAS float* sx = (LAS float*)(uintptr_t)scr;
    LAS float* cst = (LAS float*)(uintptr_t)(scr + 16384);
    LAS float* krp = (LAS float*)(uintptr_t)(scr + 16384);
    const int urow = wr * 16 + fr;

    constexpr int ZST = 1536 + 32;
    u32x2 yv[3];
    u32x4 tb0;
    {
      unsigned oy = ((b * 8 + wid) * F_ + lane * 4) * 2;
#pragma unroll
      for (int i = 0; i < 3; ++i) asm volatile("global_load_dwordx2 %0, %1, %2" : "=v"(yv[i]) : "v"(oy + i * 512), "s"(p.ws + WS_Y2));
      unsigned ok = (((tid >> 4) & 15) * 13440 + (b < 210 ? b : 0) * 64 + (tid & 15) * 4) * 4;
      asm volatile("global_load_dwordx4 %0, %1, %2" : "=v"(tb0) : "v"(ok), "s"(p.ws + WS_KVP));
    }
    auto parta_compute = [&]() {
      asm volatile("s_waitcnt vmcnt(0)" : "+v"(yv[0]), "+v"(yv[1]), "+v"(yv[2]), "+v"(tb0) :: "memory");
      float z[12];
#pragma unroll
      for (int i = 0; i < 3; ++i)
#pragma unroll
        for (int q = 0; q < 2; ++q) {
          unsigned w = pack_bf16(gelu_f(bf_lo(yv[i][q])), gelu_f(bf_hi(yv[i][q])));
          z[i * 4 + q * 2] = bf_lo(w);
          z[i * 4 + q * 2 + 1] = bf_hi(w);
        }
      float s = 0.f;
#pragma unroll
      for (int i = 0; i < 12; ++i) s += z[i];
      s = wave_sum(s);
      float mean = s * (1.f / F_), v2 = 0.f;
#pragma unroll
      for (int i = 0; i < 12; ++i) { z[i] -= mean; v2 = __builtin_fmaf(z[i], z[i], v2); }
      v2 = wave_sum(v2);
      float rstd = __builtin_amdgcn_rsqf(v2 * (1.f / F_) + 1e-5f);
#pragma unroll
      for (int i = 0; i < 3; ++i)
        *(LAS u32x2*)(uintptr_t)(scr + wid * ZST + (i * 256 + lane * 4) * 2) =
            u32x2{pack_bf16(z[i * 4] * rstd, z[i * 4 + 1] * rstd), pack_bf16(z[i * 4 + 2] * rstd, z[i * 4 + 3] * rstd)};
      if (wid < 4) *(LAS u32x4*)(krp + tid * 4) = tb0;
      WAIT_LGKM0();
    };
    auto parta_store = [&]() {
#pragma unroll
      for (int it = 0; it < 2; ++it) {
        int grp64 = it ? 8 + (wid & 3) : wid;
        int q = fr >> 2, pp = fr & 3;
        unsigned a = scr + q * ZST + (grp64 * 64 + g * 16 + pp * 4) * 2;
        bf16x8 v = cat8(lds_tr1(a), lds_tr1(a + 4 * ZST));
        int f = grp64 * 64 + lane, c = f / 96, n = f - c * 96;
        if (it == 0 || wid < 4) store16_sc1(p.ws + WS_Z2N, ((c * 256 + b) * 96 + n) * 16, __builtin_bit_cast(u32x4, v));
      }
      if (tid >= 448 && tid < 464 && b < 210) {
        int v = tid - 448;
        f32x4 t4 = {0, 0, 0, 0};
#pragma unroll
        for (int q = 0; q < 16; ++q) t4 += *(LAS f32x4*)(krp + q * 64 + v * 4);
        store16_sc1(p.ws + WS_KVR, (b * 64 + v * 4) * 4, __builtin_bit_cast(u32x4, t4));
      }
    };

    unsigned abase[2], baddr1[3], baddr2, abase3, baddr3;
    {
      int x7 = (fr >> 1) & 7;
#pragma unroll
      for (int ks = 0; ks < 2; ++ks) abase[ks] = lds0 + (wr * 16 + fr) * 128 + (((ks * 4 + g) ^ x7) << 4);
      int q = fr >> 2, pp = fr & 3;
#pragma unroll
      for (int n = 0; n < 3; ++n) {
        int k = 8 * g + q;
        baddr1[n] = lds0 + 8192 + (k * CPR + bswz<false>(k, 2 * (wc * 3 + n) + (pp >> 1))) * 16 + (pp & 1) * 8;
      }
      baddr2 = lds0 + 8192 + (g * 96 + wc * 48 + fr) * 16;
      abase3 = lds0 + (wm * 16 + fr) * 128 + (((kh * 4 + g) ^ x7) << 4);
      baddr3 = lds0 + 8192 + ((kh * 4 + g) * 80 + fr) * 16;
    }
    f32x4 acc1[3], acc2[3], cs[3], acc3[5];
#pragma unroll
    for (int n = 0; n < 3; ++n) { acc1[n] = f32x4{0, 0, 0, 0}; acc2[n] = f32x4{0, 0, 0, 0}; cs[n] = f32x4{0, 0, 0, 0}; }
#pragma unroll
    for (int n = 0; n < 5; ++n) acc3[n] = f32x4{0, 0, 0, 0};
    const short one = 0x3f80;
    bf16x8 ones = {one, one, one, one, one, one, one, one};
    asm volatile("" : "+v"(ones));

    bf16x8 fA[8], fB[8];
    unsigned slot = 0;
    int z1t = 0;
    auto adv = [&]() { slot = slot == (K2_NS - 1) * K2_SLOT ? 0 : slot + K2_SLOT; };
    auto ldf = [&](bf16x8 (&f)[8], int mode, int i, unsigned& tmp) {
      if (mode == 2) {
        if (i == 0) f[0] = lds_b128(pinv(abase3 + slot));
        else if (i < 6) { if (i == 1) tmp = pinv(baddr3 + slot); f[1 + i] = lds_b128(tmp + (i - 1) * 256); }
      } else {
        if (i == 0 || i == 4) f[i >> 2] = lds_b128(pinv(abase[i >> 2] + slot));
        else {
          int ks = i >> 2, n = (i & 3) - 1;
          if (mode == 0) {
            unsigned a = pinv(baddr1[n] + slot) + ks * 32 * CPR * 16;
            f[2 + ks * 3 + n] = cat8(lds_tr1(a), lds_tr1(a + 4 * CPR * 16));
          } else {
            if (i == 1) tmp = pinv(baddr2 + slot);
            f[2 + ks * 3 + n] = lds_b128(tmp + ks * 4 * 96 * 16 + n * 256);
          }
        }
      }
    };
    auto iter = [&](bf16x8 (&fc)[8], bf16x8 (&fn)[8], auto mc, auto mn) {
      constexpr int MC = decltype(mc)::value, MN = decltype(mn)::value;
      unsigned tmp = 0;
#pragma unroll
      for (int i = 0; i < 8; ++i) {
        if (MC == 2) {
          if (i < 5) acc3[i] = mfma16(fc[2 + i], fc[0], acc3[i]);
        } else if (i < 6) {
          int ks = i / 3, n = i % 3;
          if (MC == 0) acc1[n] = mfma16(fc[2 + i], fc[ks], acc1[n]);
          else acc2[n] = mfma16(fc[2 + i], fc[ks], acc2[n]);
        }
        if (MN < 3) ldf(fn, MN, i, tmp);
        SBAR();
      }
      if (MC == 0) {
        if (wr == (z1t & 3)) {
#pragma unroll
          for (int i = 0; i < 6; ++i) cs[i % 3] = mfma16(fc[2 + i], ones, cs[i % 3]);
        }
        ++z1t;
        SBAR();
      }
      if (MN < 3) adv();
    };
    using I0 = std::integral_constant<int, 0>;
    using I1 = std::integral_constant<int, 1>;
    using I2 = std::integral_constant<int, 2>;
    using I3 = std::integral_constant<int, 3>;
#define FLAG_ADD(off) do { if (tid == 0) __hip_atomic_fetch_add((gu32*)(flags + (off)), 1u, __ATOMIC_RELAXED, __HIP_MEMORY_SCOPE_AGENT); } while (0)
    const f32x2 xs = *(const f32x2*)((const float*)(p.ws + WS_XST) + (size_t)(rg * 64 + urow) * 2);
    const float bias = bf_lo(p.b_sp[rg * 64 + urow]);
    parta_compute();
    STAMP(1, 209);
    BARRIER();
    STAMP(1, 0);
    parta_store();
    {
      unsigned tmp = 0;
#pragma unroll
      for (int i = 0; i < 8; ++i) ldf(fA, 0, i, tmp);
      adv();
    }
    BARRIER();
    iter(fA, fB, I0{}, I0{}); WAIT_VM(0); BARRIER();
    FLAG_ADD((b & 15) * 32);
    STAMP(1, 1);
    iter(fB, fA, I0{}, I0{}); BARRIER();
    iter(fA, fB, I0{}, I0{}); BARRIER();
    iter(fB, fA, I0{}, I0{}); BARRIER();
    STAMP(1, 4);
    iter(fA, fB, I0{}, I0{}); BARRIER();
    iter(fB, fA, I0{}, I0{}); BARRIER();
    STAMP(1, 6);
    iter(fA, fB, I0{}, I0{}); BARRIER();
    iter(fB, fA, I0{}, I0{}); BARRIER();
    iter(fA, fB, I0{}, I0{}); BARRIER();
    iter(fB, fA, I0{}, I1{}); BARRIER();
    STAMP(1, 10);
#pragma unroll 1
    for (int t = 10; t < 18; t += 2) {
      iter(fA, fB, I1{}, I1{}); BARRIER();
      iter(fB, fA, I1{}, I1{}); BARRIER();
    }
    STAMP(1, 18);
    iter(fA, fB, I1{}, I1{});
    if (fr == 0) {
#pragma unroll
      for (int n = 0; n < 3; ++n) *(LAS f32x4*)(cst + wr * 96 + wc * 48 + n * 16 + g * 4) = cs[n];
    }
    WAIT_LGKM0();
    BARRIER();
    auto z1act = [&](int n) {
      SBAR();
      int c0 = wc * 48 + n * 16 + g * 4;
      f32x4 c4 = (*(LAS f32x4*)(cst + c0) + *(LAS f32x4*)(cst + 96 + c0)) + (*(LAS f32x4*)(cst + 192 + c0) + *(LAS f32x4*)(cst + 288 + c0));
#pragma unroll
      for (int r = 0; r < 4; ++r) acc1[n][r] = gelu_f(__builtin_fmaf(acc1[n][r], xs[1], c4[r] * xs[0]));
      asm volatile("" : "+v"(acc1[n]));
      SBAR();
    };
    iter(fB, fA, I1{}, I1{}); z1act(0); BARRIER();
    iter(fA, fB, I1{}, I1{}); z1act(1); BARRIER();
    iter(fB, fA, I1{}, I1{}); z1act(2); BARRIER();
    iter(fA, fB, I1{}, I1{}); BARRIER();
    u32x4 xv0, xv1;
    {
      int i0 = tid, i1 = tid < 128 ? tid + 512 : tid;
      xv0 = *(const u32x4*)(p.x + (size_t)(rg3 * 64 + i0 / 10) * D_ + cb3 * 80 + (i0 % 10) * 8);
      xv1 = *(const u32x4*)(p.x + (size_t)(rg3 * 64 + i1 / 10) * D_ + cb3 * 80 + (i1 % 10) * 8);
    }
    iter(fB, fA, I1{}, I1{}); BARRIER();
#pragma unroll 1
    for (int t = 24; t < 34; t += 2) {
      iter(fA, fB, I1{}, I1{}); BARRIER();
      iter(fB, fA, I1{}, I1{}); BARRIER();
    }
    iter(fA, fB, I1{}, I1{}); BARRIER();
    iter(fB, fA, I1{}, I1{}); BARRIER();
#pragma unroll 1
    for (int t = 36; t < 40; t += 2) {
      iter(fA, fB, I1{}, I1{}); BARRIER();
      iter(fB, fA, I1{}, I1{}); BARRIER();
    }
    iter(fA, fB, I1{}, I1{}); BARRIER();
    iter(fB, fA, I1{}, I2{});
    STAMP(1, 42);
    constexpr int OST = 96 * 2 + 16;
    {
      unsigned dst = scr + urow * OST + (wc * 48 + g * 4) * 2;
#pragma unroll
      for (int n = 0; n < 3; ++n) {
        float v[4];
#pragma unroll
        for (int r = 0; r < 4; ++r) v[r] = acc1[n][r] * (acc2[n][r] + bias);
        *(LAS u32x2*)(uintptr_t)(dst + n * 32) = u32x2{pack_bf16(v[0], v[1]), pack_bf16(v[2], v[3])};
      }
    }
    SBAR();
    WAIT_LGKM0();
    BARRIER();
    iter(fA, fB, I2{}, I2{}); BARRIER();
    STAMP(1, 43);
    iter(fB, fA, I2{}, I2{}); BARRIER();
    iter(fA, fB, I2{}, I2{}); BARRIER();
    iter(fB, fA, I2{}, I2{}); BARRIER();
    STAMP(1, 46);
#pragma unroll 1
    for (int t = 46; t < 50; t += 2) {
      iter(fA, fB, I2{}, I2{}); BARRIER();
      iter(fB, fA, I2{}, I2{}); BARRIER();
    }
    iter(fA, fB, I2{}, I2{}); BARRIER();
    STAMP(1, 51);
    iter(fB, fA, I2{}, I3{});
    BARRIER();
    STAMP(1, 52);
    BARRIER();
    {
      unsigned tmp = 0;
#pragma unroll
      for (int i = 0; i < 8; ++i) ldf(fA, 2, i, tmp);
      adv();
    }
    BARRIER();
    STAMP(1, 53);
#pragma unroll 1
    for (int t = T_B; t < T_END; t += 2) {
      iter(fA, fB, I2{}, I2{}); BARRIER();
      iter(fB, fA, I2{}, I2{});
      if (t == T_END - 2) WAIT_LGKM0();
      BARRIER();
    }
    STAMP(1, 100);
    constexpr int OS3 = 80 * 4 + 16;
    {
      unsigned dst = lds0 + kh * (64 * OS3) + (wm * 16 + fr) * OS3 + g * 16;
#pragma unroll
      for (int n = 0; n < 5; ++n) *(LAS f32x4*)(uintptr_t)(dst + n * 64) = acc3[n];
    }
    WAIT_LGKM0();
    BARRIER();
#pragma unroll
    for (int it = 0; it < 2; ++it) {
      int idx = tid + it * 512;
      if (idx < 640) {
        int row = idx / 10, c = idx - row * 10;
        u32x4 xv = it ? xv1 : xv0;
        unsigned la = lds0 + row * OS3 + c * 32;
        f32x4 a = *(LAS f32x4*)(uintptr_t)(la) + *(LAS f32x4*)(uintptr_t)(la + 64 * OS3);
        f32x4 c4 = *(LAS f32x4*)(uintptr_t)(la + 16) + *(LAS f32x4*)(uintptr_t)(la + 64 * OS3 + 16);
        u32x4 o = {pack_bf16(bf_lo(xv[0]) + a[0], bf_hi(xv[0]) + a[1]), pack_bf16(bf_lo(xv[1]) + a[2], bf_hi(xv[1]) + a[3]),
                   pack_bf16(bf_lo(xv[2]) + c4[0], bf_hi(xv[2]) + c4[1]), pack_bf16(bf_lo(xv[3]) + c4[2], bf_hi(xv[3]) + c4[3])};
        *(u32x4*)(p.out + (size_t)(rg3 * 64 + row) * D_ + cb3 * 80 + c * 8) = o;
      }
    }
    STAMP(1, 201);
  }
}

extern "C" void kernel_launch(void* const* d_in, const int* in_sizes, int n_in, void* d_out, int out_size, void* d_ws,
                              size_t ws_size, hipStream_t stream) {
  Params p;
  p.x = (const u16*)d_in[0];
  p.wqkv = (const u16*)d_in[1];
  p.wo = (const u16*)d_in[2];
  p.w_in = (const u16*)d_in[4];
  p.w_sp = (const u16*)d_in[5];
  p.b_sp = (const u16*)d_in[6];
  p.w_out = (const u16*)d_in[7];
  p.out = (u16*)d_out;
  p.ws = (char*)d_ws;
  (void)hipFuncSetAttribute((const void*)stage1_kernel, hipFuncAttributeMaxDynamicSharedMemorySize, S1_LDS);
  (void)hipFuncSetAttribute((const void*)stage2_kernel, hipFuncAttributeMaxDynamicSharedMemorySize, K2_LDS);
  stage1_kernel<<<256, S1_THREADS, S1_LDS, stream>>>(p);
  stage2_kernel<<<256, K2_THREADS, K2_LDS, stream>>>(p);
}

#if defined(__HIP_DEVICE_COMPILE__) && __HIP_DEVICE_COMPILE__
#pragma clang attribute pop
#endif
```

```cpp
#if defined(__HIP_DEVICE_COMPILE__) && __HIP_DEVICE_COMPILE__
#pragma clang attribute push(__attribute__((target("no-packed-fp32-ops"))), apply_to = function)
#endif
#include <hip/hip_runtime.h>
#include <hip/hip_bf16.h>
#include <stdint.h>
#include <type_traits>

typedef short bf16x8 __attribute__((ext_vector_type(8)));
typedef short s16x4 __attribute__((ext_vector_type(4)));
typedef float f32x4 __attribute__((ext_vector_type(4)));
typedef float f32x2 __attribute__((ext_vector_type(2)));
typedef unsigned u32x4 __attribute__((ext_vector_type(4)));
typedef unsigned u32x2 __attribute__((ext_vector_type(2)));
typedef __bf16 bf16x2_t __attribute__((ext_vector_type(2)));
typedef unsigned short u16;

#define TIDX ((int)__builtin_amdgcn_workitem_id_x())
#define BIDX ((int)__builtin_amdgcn_workgroup_id_x())
#define LAS __attribute__((address_space(3)))
#define DI __device__ __forceinline__

constexpr int S_ = 2048, D_ = 640, H_ = 32, DH_ = 20, F_ = 768, KA_ = D_ + F_;

constexpr size_t WS_Y2 = 0;
constexpr size_t WS_Q = WS_Y2 + (size_t)S_ * F_ * 2;
constexpr size_t WS_UA = WS_Q + (size_t)S_ * D_ * 2;
constexpr size_t WS_B3 = WS_UA + (size_t)S_ * KA_ * 2;
constexpr size_t WS_KVP = WS_B3 + (size_t)KA_ * D_ * 2;
constexpr size_t WS_XST = WS_KVP + (size_t)16 * 32 * 420 * 4;
constexpr size_t WS_Z2N = WS_XST + (size_t)S_ * 2 * 4;
constexpr size_t WS_END = WS_Z2N + (size_t)S_ * F_ * 2;
constexpr size_t WS_FLAGS = WS_END;
constexpr size_t WS_KVR = WS_FLAGS + 64 * 32 * 4;

DI unsigned pack_bf16(float a, float b) {
  f32x2 v = {a, b};
  bf16x2_t r = __builtin_convertvector(v, bf16x2_t);
  return __builtin_bit_cast(unsigned, r);
}
DI float bf_lo(unsigned w) { return __builtin_bit_cast(float, w << 16); }
DI float bf_hi(unsigned w) { return __builtin_bit_cast(float, w & 0xffff0000u); }
DI float phi_f(float t) {
  return (fmaxf(t, 0.f) + 1e-6f) + __builtin_amdgcn_exp2f(fminf(t, 0.f) * 1.4426950408889634f);
}
DI float gelu_f(float x) {
  constexpr float c1 = -2.f * 0.7978845608028654f * 1.4426950408889634f;
  constexpr float c3 = c1 * 0.044715f;
  float a = x * __builtin_fmaf(x * x, c3, c1);
  return x * __builtin_amdgcn_rcpf(1.f + __builtin_amdgcn_exp2f(a));
}
DI float wave_sum(float v) {
  v += __builtin_bit_cast(float, __builtin_amdgcn_update_dpp(0, __builtin_bit_cast(int, v), 0xB1, 0xf, 0xf, false));
  v += __builtin_bit_cast(float, __builtin_amdgcn_update_dpp(0, __builtin_bit_cast(int, v), 0x4E, 0xf, 0xf, false));
  v += __builtin_bit_cast(float, __builtin_amdgcn_update_dpp(0, __builtin_bit_cast(int, v), 0x141, 0xf, 0xf, false));
  v += __builtin_bit_cast(float, __builtin_amdgcn_update_dpp(0, __builtin_bit_cast(int, v), 0x140, 0xf, 0xf, false));
  int iv = __builtin_bit_cast(int, v);
  float r0 = __builtin_bit_cast(float, __builtin_amdgcn_readlane(iv, 0)), r1 = __builtin_bit_cast(float, __builtin_amdgcn_readlane(iv, 16));
  float r2 = __builtin_bit_cast(float, __builtin_amdgcn_readlane(iv, 32)), r3 = __builtin_bit_cast(float, __builtin_amdgcn_readlane(iv, 48));
  return (r0 + r1) + (r2 + r3);
}
DI f32x4 mfma16(bf16x8 a, bf16x8 b, f32x4 c) { return __builtin_amdgcn_mfma_f32_16x16x32_bf16(a, b, c, 0, 0, 0); }
DI bf16x8 lds_b128(unsigned addr) { return *(LAS bf16x8*)(uintptr_t)addr; }
DI s16x4 lds_tr1(unsigned a) { return __builtin_amdgcn_ds_read_tr16_b64_v4i16((LAS s16x4*)(uintptr_t)a); }
DI bf16x8 cat8(s16x4 lo, s16x4 hi) { return __builtin_shufflevector(lo, hi, 0, 1, 2, 3, 4, 5, 6, 7); }
DI void glds16(const void* sbase, unsigned voff, unsigned lds_addr) {
  unsigned sv;
  asm volatile("s_mov_b32 %0, m0\n\ts_mov_b32 m0, %3\n\ts_nop 0\n\tglobal_load_lds_dwordx4 %1, %2\n\ts_mov_b32 m0, %0"
               : "=&s"(sv) : "v"(voff), "s"(sbase), "s"(lds_addr));
}
DI void touch_line(unsigned& sink, const void* sbase, unsigned voff) {
  asm volatile("global_load_dword %0, %1, %2" : "=v"(sink) : "v"(voff), "s"(sbase));
}
#define WAIT_VM(n) asm volatile("s_waitcnt vmcnt(%0)" ::"i"(n) : "memory")
#define WAIT_LGKM0() asm volatile("s_waitcnt lgkmcnt(0)" ::: "memory")
#define BARRIER() do { asm volatile("" ::: "memory"); __builtin_amdgcn_s_barrier(); asm volatile("" ::: "memory"); } while (0)
#define SBAR() __builtin_amdgcn_sched_barrier(0)
DI unsigned pinv(unsigned a) { asm volatile("" : "+v"(a)); return a; }

#define STAMP(kid, idx)

struct Params {
  const u16* x;
  const u16* wqkv;
  const u16* wo;
  const u16* w_in;
  const u16* w_sp;
  const u16* b_sp;
  const u16* w_out;
  u16* out;
  char* ws;
};

template <bool XQ>
DI int bswz(int k, int c) { return c ^ (((k >> 3) & 1) << 1) ^ (XQ ? (((k >> 1) & 1) << 2) : 0); }

constexpr int S1_STAGE = 16384 + 24576;
constexpr int S1_NBUF = 4;
constexpr int S1_LDS = S1_NBUF * S1_STAGE;
constexpr int S1_THREADS = 768;

template <int TYPE>
DI void stage1_body(const Params& p, int rg, int ct, unsigned lds0, int xcd, int jb) {
  constexpr int BN = TYPE == 2 ? 192 : 160;
  constexpr int CPR = BN / 8;
  constexpr int NF = BN / 32;
  constexpr int BI = BN / 32;
  constexpr int G = 4 + BI;
  constexpr int NT = D_ / 64;
  constexpr bool XQ = TYPE == 2;
  const int tid = TIDX, lane = tid & 63;
  const int wid = __builtin_amdgcn_readfirstlane(tid >> 6);
  const int fr = lane & 15, g = lane >> 4;
  const int wr = (wid >> 1) & 3, wc = wid & 1;

  LAS float* st_rs = (LAS float*)(uintptr_t)lds0;
  LAS float* st_sq = st_rs + 128;
  LAS float* st_cs = (LAS float*)(uintptr_t)(lds0 + 1024);
  const unsigned otile = lds0 + 4096;
  constexpr int OST = BN * 2 + 16;

  if (wid >= 8) {
    const int lw = wid - 8;
    const u16* Bm = TYPE == 2 ? p.w_in : p.wqkv;
    constexpr int ldb = TYPE == 2 ? 2 * F_ : 3 * D_;
    unsigned aoff, boff[BI];
    {
      int row = lw * 8 + (lane >> 3), c = (lane & 7) ^ ((row >> 1) & 7);
      aoff = ((rg * 128 + row) * D_ + c * 8) * 2;
    }
#pragma unroll
    for (int i = 0; i < BI; ++i) {
      int slot = (i * 4 + lw) * 64 + lane;
      int k = slot / CPR, c = bswz<XQ>(k, slot - k * CPR);
      int col;
      if (TYPE == 0) col = c < 10 ? (D_ + ct * 80 + c * 8) : (2 * D_ + ct * 80 + (c - 10) * 8);
      else if (TYPE == 1) col = ct * 160 + c * 8;
      else col = F_ + ct * 192 + c * 8;
      boff[i] = (k * ldb + col) * 2;
    }
    auto issue = [&](int t) {
      unsigned base = lds0 + (t & 3) * S1_STAGE;
#pragma unroll
      for (int i = 0; i < 4; ++i) glds16(p.x + t * 64 + i * 32 * D_, aoff, base + (i * 4 + lw) * 1024);
#pragma unroll
      for (int i = 0; i < BI; ++i) glds16(Bm + (size_t)t * 64 * ldb, boff[i], base + 16384 + (i * 4 + lw) * 1024);
    };
    issue(0);
    issue(1);
    unsigned sk[5];
    WAIT_VM(G);
    BARRIER();
    issue(2);
    WAIT_VM(G);
    BARRIER();
#pragma unroll 1
    for (int t = 0; t < NT - 4; ++t) {
      issue(t + 3);
      WAIT_VM(G);
      BARRIER();
    }
    issue(NT - 1);
    WAIT_VM(G);
    BARRIER();
    WAIT_VM(0);
    BARRIER();
    {
      const int lt = lw * 64 + lane;
      touch_line(sk[0], p.w_sp, (((xcd & 3) * 32 + jb) * 512 + lt) * 128);
      touch_line(sk[1], p.w_sp, (((xcd & 3) * 32 + jb) * 512 + 256 + lt) * 128);
      int i = lt < 120 ? lt : 0;
      touch_line(sk[2], p.w_in, ((jb * 20 + i / 6) * (2 * F_) + (xcd >> 2) * 384) * 2 + (i % 6) * 128);
      int i2 = (jb * 8 + xcd) * 30 + (lt < 30 ? lt : 0);
      int i3 = (jb * 8 + xcd) * 25 + (lt < 25 ? lt : 0);
      touch_line(sk[3], p.w_out, i2 * 128);
      touch_line(sk[4], p.wo, i3 * 128);
    }
    BARRIER();
    BARRIER();
    BARRIER();
    BARRIER();
    asm volatile("s_waitcnt vmcnt(0)" : "+v"(sk[0]), "+v"(sk[1]), "+v"(sk[2]), "+v"(sk[3]), "+v"(sk[4]) :: "memory");
    return;
  } else {
    unsigned sink;
    {
      const int kt = wid + 2, cq = xcd >> 1;
      const char* a;
      if (lane < 32) {
        a = (const char*)p.x + (size_t)((xcd & 1) * 1024 + jb * 32 + lane) * (D_ * 2) + kt * 128;
      } else {
        int i = jb * 30 + (lane < 62 ? lane - 32 : 0), row = kt * 64 + i / 15, sl = i % 15;
        a = sl < 12 ? (const char*)p.wqkv + (((row * (3 * D_) + (sl >> 2) * D_ + cq * 160) * 2 + ((sl & 3) < 3 ? (sl & 3) * 128 : 318)) & ~3)
                    : (const char*)p.w_in + (row * (2 * F_) + F_ + cq * 192) * 2 + (sl - 12) * 128;
      }
      asm volatile("global_load_dword %0, %1, off" : "=v"(sink) : "v"(a));
    }
    unsigned abase[2], baddr[NF];
    {
      int x7 = (fr >> 1) & 7;
#pragma unroll
      for (int ks = 0; ks < 2; ++ks) abase[ks] = lds0 + (wr * 32 + wc * 16 + fr) * 128 + (((ks * 4 + g) ^ x7) << 4);
      int q = fr >> 2, pp = fr & 3;
#pragma unroll
      for (int n = 0; n < NF; ++n) {
        int c;
        if (TYPE == 0) {
          int w = 2 * n + (pp >> 1);
          c = w < 5 ? wc * 5 + w : 10 + wc * 5 + (w - 5);
        } else {
          c = 2 * (wc * NF + n) + (pp >> 1);
        }
        int k = 8 * g + q;
        baddr[n] = lds0 + 16384 + (k * CPR + bswz<XQ>(k, c)) * 16 + (pp & 1) * 8;
      }
    }
    const int am1 = wc ? -2048 : 2048;
    f32x4 acc[2][NF], cs[NF], rs, sq;
#pragma unroll
    for (int m = 0; m < 2; ++m)
#pragma unroll
      for (int n = 0; n < NF; ++n) acc[m][n] = f32x4{0, 0, 0, 0};
#pragma unroll
    for (int n = 0; n < NF; ++n) cs[n] = f32x4{0, 0, 0, 0};
    rs = f32x4{0, 0, 0, 0};
    sq = f32x4{0, 0, 0, 0};
    const short one = 0x3f80;
    bf16x8 ones = {one, one, one, one, one, one, one, one};
    asm volatile("" : "+v"(ones));

    bf16x8 af0[2], af1[2];
    s16x4 bl0[NF], bh0[NF], bl1[NF], bh1[NF];
    auto phase = [&](bf16x8 (&afc)[2], s16x4 (&blc)[NF], s16x4 (&bhc)[NF], bf16x8 (&afn)[2], s16x4 (&bln)[NF], s16x4 (&bhn)[NF],
                     unsigned sbn, int ksn, bool docs) {
      const unsigned a0 = pinv(abase[ksn] + sbn);
      unsigned ba = 0;
#pragma unroll
      for (int i = 0; i < 2 * NF + 2; ++i) {
        if (i < 2 * NF) {
          int m = i / NF, n = i % NF;
          bf16x8 bq = cat8(blc[n], bhc[n]);
          acc[m][n] = TYPE == 0 ? mfma16(afc[m], bq, acc[m][n]) : mfma16(bq, afc[m], acc[m][n]);
        } else if (i == 2 * NF) rs = mfma16(afc[0], ones, rs);
        else sq = mfma16(afc[0], afc[0], sq);
        if (i == 0) afn[0] = lds_b128(a0);
        else if (i <= 2 * NF) {
          int r = i - 1;
          if (r & 1) bhn[r >> 1] = lds_tr1(ba + (ksn * 32 + 4) * CPR * 16);
          else { ba = pinv(baddr[r >> 1] + sbn); bln[r >> 1] = lds_tr1(ba + ksn * 32 * CPR * 16); }
        } else afn[1] = lds_b128(a0 + am1);
        SBAR();
      }
      if (docs) {
#pragma unroll
        for (int n = 0; n < NF; ++n) cs[n] = mfma16(ones, cat8(blc[n], bhc[n]), cs[n]);
      }
      SBAR();
    };
    STAMP(0, 0);
    BARRIER();
    af0[0] = lds_b128(abase[0]);
    af0[1] = lds_b128(abase[0] + am1);
#pragma unroll
    for (int n = 0; n < NF; ++n) { bl0[n] = lds_tr1(baddr[n]); bh0[n] = lds_tr1(baddr[n] + 4 * CPR * 16); }
    phase(af0, bl0, bh0, af1, bl1, bh1, 0, 1, wr == 0);
    BARRIER();
    phase(af1, bl1, bh1, af0, bl0, bh0, S1_STAGE, 0, wr == 1);
    BARRIER();
#pragma unroll 1
    for (int t = 1; t < NT; ++t) {
      STAMP(0, 1 + t);
      phase(af0, bl0, bh0, af1, bl1, bh1, (t & 3) * S1_STAGE, 1, wr == ((2 * t) & 3));
      phase(af1, bl1, bh1, af0, bl0, bh0, ((t + 1) & 3) * S1_STAGE, 0, wr == ((2 * t + 1) & 3));
      if (t == NT - 1) WAIT_LGKM0();
      BARRIER();
    }
    STAMP(0, 100);
    asm volatile("s_waitcnt vmcnt(0)" : "+v"(sink) :: "memory");
    {
      int r0 = wr * 32 + wc * 16;
      if (fr == 0) *(LAS f32x4*)(st_rs + r0 + g * 4) = rs;
      if ((fr >> 2) == g) {
        int r = fr & 3;
        st_sq[r0 + fr] = r == 0 ? sq[0] : r == 1 ? sq[1] : r == 2 ? sq[2] : sq[3];
      }
      if (g == 0) {
#pragma unroll
        for (int n = 0; n < NF; ++n) st_cs[wr * 192 + wc * NF * 16 + n * 16 + fr] = cs[n][0];
      }
    }
    WAIT_LGKM0();
    BARRIER();
    STAMP(0, 102);
    if (TYPE == 0) {
      float csv[NF];
#pragma unroll
      for (int n = 0; n < NF; ++n) {
        int c0 = wc * NF * 16 + n * 16 + fr;
        csv[n] = (st_cs[c0] + st_cs[192 + c0]) + (st_cs[384 + c0] + st_cs[576 + c0]);
      }
      bf16x8 xf[NF];
#pragma unroll
      for (int m = 0; m < 2; ++m) {
        int r0 = wr * 32 + (m ^ wc) * 16 + g * 4;
        f32x4 mean = *(LAS f32x4*)(st_rs + r0) * (1.f / D_);
        f32x4 ex2 = *(LAS f32x4*)(st_sq + r0) * (1.f / D_);
        f32x4 rstd, nm;
#pragma unroll
        for (int r = 0; r < 4; ++r) {
          rstd[r] = __builtin_amdgcn_rsqf(fmaxf(ex2[r] - mean[r] * mean[r], 0.f) + 1e-5f);
          nm[r] = -mean[r] * rstd[r];
        }
#pragma unroll
        for (int n = 0; n < NF; ++n) {
          f32x4 y = acc[m][n] * rstd + nm * csv[n];
          bool isk = (n * 16 + fr) < 40;
          float v[4];
#pragma unroll
          for (int r = 0; r < 4; ++r) v[r] = n < 2 ? phi_f(y[r]) : (n == 2 ? (isk ? phi_f(y[r]) : y[r]) : y[r]);
          u32x4 t4 = __builtin_bit_cast(u32x4, xf[n]);
          t4[m * 2] = pack_bf16(v[0], v[1]);
          t4[m * 2 + 1] = pack_bf16(v[2], v[3]);
          xf[n] = __builtin_bit_cast(bf16x8, t4);
        }
      }
      SBAR();
      STAMP(0, 104);
      constexpr int PI[7] = {0, 0, 1, 1, 1, 2, 2};
      constexpr int PJ[7] = {2, 3, 2, 3, 4, 3, 4};
      f32x4 dd[10];
#pragma unroll
      for (int q = 0; q < 7; ++q) dd[q] = mfma16(xf[PI[q]], xf[PJ[q]], f32x4{0, 0, 0, 0});
#pragma unroll
      for (int i = 0; i < 3; ++i) dd[7 + i] = mfma16(xf[i], ones, f32x4{0, 0, 0, 0});
      SBAR();
#pragma unroll
      for (int q = 0; q < 10; ++q) *(LAS f32x4*)(uintptr_t)(otile + wid * 10240 + q * 1024 + lane * 16) = dd[q];
    } else {
      f32x4 csv[NF];
#pragma unroll
      for (int n = 0; n < NF; ++n) {
        int c0 = wc * NF * 16 + n * 16 + g * 4;
        csv[n] = (*(LAS f32x4*)(st_cs + c0) + *(LAS f32x4*)(st_cs + 192 + c0)) + (*(LAS f32x4*)(st_cs + 384 + c0) + *(LAS f32x4*)(st_cs + 576 + c0));
      }
#pragma unroll
      for (int m = 0; m < 2; ++m) {
        int rl = wr * 32 + (m ^ wc) * 16 + fr;
        float mean = st_rs[rl] * (1.f / D_);
        float ex2 = st_sq[rl] * (1.f / D_);
        float rstd = __builtin_amdgcn_rsqf(fmaxf(ex2 - mean * mean, 0.f) + 1e-5f);
        float nm = -mean * rstd;
        if (TYPE == 2 && ct == 0 && m == 0 && g == 0) *(f32x2*)((float*)(p.ws + WS_XST) + (size_t)(rg * 128 + rl) * 2) = f32x2{nm, rstd};
        unsigned dst = otile + rl * OST + (wc * NF * 16 + g * 4) * 2;
#pragma unroll
        for (int n = 0; n < NF; ++n) {
          f32x4 y = acc[m][n] * rstd + csv[n] * nm;
          *(LAS u32x2*)(uintptr_t)(dst + n * 32) = u32x2{pack_bf16(y[0], y[1]), pack_bf16(y[2], y[3])};
        }
      }
    }
    STAMP(0, 105);
  }
  WAIT_LGKM0();
  BARRIER();
  STAMP(0, 106);
  if (TYPE == 0) {
    float* kvp = (float*)(p.ws + WS_KVP) + ((size_t)rg * 32 + ct * 4) * 420;
    for (int i = tid; i < 1680; i += 512) {
      int hl = i / 420, w = i - hl * 420;
      int hi = hl & 1, ci, cj, q;
      if (w < 400) {
        int d = w / 20, e = w - d * 20;
        ci = hi * 20 + d; cj = 40 + hi * 20 + e;
        int pi = ci >> 4, pj = cj >> 4;
        q = pi == 0 ? pj - 2 : (pi == 1 ? pj : pj + 2);
      } else {
        ci = hi * 20 + (w - 400); cj = 0;
        q = 7 + (ci >> 4);
      }
      unsigned a = otile + (hl >> 1) * 10240 + q * 1024 + ((((ci & 15) >> 2) * 16 + (cj & 15)) * 4 + (ci & 3)) * 4;
      float v = (*(LAS float*)(uintptr_t)(a) + *(LAS float*)(uintptr_t)(a + 2 * 10240)) +
                (*(LAS float*)(uintptr_t)(a + 4 * 10240) + *(LAS float*)(uintptr_t)(a + 6 * 10240));
      kvp[i] = v;
    }
  } else {
    u16* dstm = TYPE == 1 ? (u16*)(p.ws + WS_Q) + (size_t)rg * 128 * D_ + ct * 160
                          : (u16*)(p.ws + WS_Y2) + (size_t)rg * 128 * F_ + ct * 192;
    constexpr int ldo = TYPE == 1 ? D_ : F_;
    for (int idx = tid; idx < 128 * CPR; idx += 512) {
      int row = idx / CPR, c = idx - row * CPR;
      u32x4 v = *(LAS u32x4*)(uintptr_t)(otile + row * OST + c * 16);
      *(u32x4*)(dstm + (size_t)row * ldo + c * 8) = v;
    }
  }
}

__global__ __launch_bounds__(S1_THREADS, 3) void stage1_kernel(Params p) {
  extern __shared__ __attribute__((aligned(16))) char shm[];
  unsigned lds0 = (unsigned)(uintptr_t)(LAS char*)shm;
  int b = BIDX, xcd = b & 7, j = b >> 3;
  int rg = (xcd & 1) * 8 + (j & 7);
  int cq = xcd >> 1, ci = j >> 3;
  STAMP(0, 200);
  if (b == 0) {
    for (int i = TIDX; i < 64 * 32; i += S1_THREADS) ((unsigned*)(p.ws + WS_FLAGS))[i] = 0u;
  }
  if (ci < 2) stage1_body<0>(p, rg, cq * 2 + ci, lds0, xcd, j);
  else if (ci == 2) stage1_body<1>(p, rg, cq, lds0, xcd, j);
  else stage1_body<2>(p, rg, cq, lds0, xcd, j);
  STAMP(0, 201);
}

constexpr int K2_SLOT = 8192 + 12288;
constexpr int K2_NS = 7;
constexpr int K2_SCR = K2_NS * K2_SLOT;
constexpr int K2_LDS = K2_SCR + 20480;
constexpr int K2_MID = (K2_NS - 1) * K2_SLOT;
constexpr int K2_THREADS = 1024;
constexpr int T_G = 10, T_A = 42, T_B = 52, T_END = 64;
constexpr unsigned SPIN_CAP = 1u << 22;

typedef __attribute__((address_space(1))) unsigned gu32;
DI void* uni_ptr(const void* q) {
  unsigned long long a = (unsigned long long)q;
  unsigned lo = __builtin_amdgcn_readfirstlane((unsigned)a), hi = __builtin_amdgcn_readfirstlane((unsigned)(a >> 32));
  return (void*)(((unsigned long long)hi << 32) | lo);
}
DI void store16_sc1(void* sbase, unsigned voff, u32x4 v) {
  sbase = uni_ptr(sbase);
  asm volatile("s_nop 4\n\tglobal_store_dwordx4 %0, %1, %2 sc1\n\ts_nop 1" ::"v"(voff), "v"(v), "s"(sbase) : "memory");
}
DI void store8_sc1(void* sbase, unsigned voff, u32x2 v) {
  asm volatile("global_store_dwordx2 %0, %1, %2 sc1\n\ts_nop 1" ::"v"(voff), "v"(v), "s"(sbase) : "memory");
}
DI void glds16_sc1(const void* sbase, unsigned voff, unsigned lds_addr) {
  unsigned sv;
  asm volatile("s_mov_b32 %0, m0\n\ts_mov_b32 m0, %3\n\ts_nop 0\n\tglobal_load_lds_dwordx4 %1, %2 sc1\n\ts_mov_b32 m0, %0"
               : "=&s"(sv) : "v"(voff), "s"(sbase), "s"(lds_addr));
}
DI void wait_flags(unsigned* flags, int nw, unsigned target, int lane) {
  gu32* fp = (gu32*)(flags + (lane < nw ? lane : 0) * 32);
  for (unsigned spins = 0; spins < SPIN_CAP; ++spins) {
    unsigned v = __hip_atomic_load(fp, __ATOMIC_RELAXED, __HIP_MEMORY_SCOPE_AGENT);
    if (__all(v >= target)) break;
    __builtin_amdgcn_s_sleep(1);
  }
  __builtin_amdgcn_fence(__ATOMIC_ACQUIRE, "wavefront");
}

__global__ __launch_bounds__(K2_THREADS, 4) void stage2_kernel(Params p) {
  extern __shared__ __attribute__((aligned(16))) char shm[];
  const unsigned lds0 = (unsigned)(uintptr_t)(LAS char*)shm;
  const int b = BIDX, xcd = b & 7, j = b >> 3;
  const int rg = (xcd & 3) * 8 + (j & 7);
  const int cb = (xcd >> 2) * 4 + (j >> 3);
  const int rg3 = xcd * 4 + (j & 3), cb3 = j >> 2;
  const int tid = TIDX, lane = tid & 63;
  const int wid = __builtin_amdgcn_readfirstlane(tid >> 6);
  const int fr = lane & 15, g = lane >> 4;
  unsigned* flags = (unsigned*)(p.ws + WS_FLAGS);
  constexpr int CPR = 12;
  STAMP(1, 200);

  if (wid >= 12) {
    const int st = tid - 768, sw = wid - 12;
    const int mcb = b & 7;
    const unsigned scr = lds0 + K2_SCR;
    LAS float* zt = (LAS float*)(uintptr_t)(scr + 18432);
#define SB(n) do { _Pragma("unroll 1") for (int i_ = 0; i_ < (n); ++i_) BARRIER(); } while (0)
#define FLAG_ADD_S(off) do { if (st == 0) __hip_atomic_fetch_add((gu32*)(flags + (off)), 1u, __ATOMIC_RELAXED, __HIP_MEMORY_SCOPE_AGENT); } while (0)
    u32x4 w0, w1;
    u32x2 qw[5];
    {
      const u16* wsrc = b < 128 ? p.wo + (size_t)(b >> 3) * 40 * D_ : p.w_out + (size_t)((b - 128) >> 3) * 48 * D_;
      const int nch = b < 128 ? 400 : 480;
      int i1 = st + 256 < nch ? st + 256 : 0;
      w0 = *(const u32x4*)(wsrc + (st / 10) * D_ + mcb * 80 + (st % 10) * 8);
      w1 = *(const u32x4*)(wsrc + (i1 / 10) * D_ + mcb * 80 + (i1 % 10) * 8);
      const u16* qsrc = (const u16*)(p.ws + WS_Q) + (size_t)((b >> 1) * 16 + (st >> 4)) * D_ + (b & 1) * 320 + (st & 15) * 20;
#pragma unroll
      for (int q = 0; q < 5; ++q) qw[q] = *(const u32x2*)(qsrc + q * 4);
    }
    SB(3);
    float pq[20];
#pragma unroll
    for (int q = 0; q < 3; ++q) {
      pq[q * 4] = phi_f(bf_lo(qw[q][0])); pq[q * 4 + 1] = phi_f(bf_hi(qw[q][0]));
      pq[q * 4 + 2] = phi_f(bf_lo(qw[q][1])); pq[q * 4 + 3] = phi_f(bf_hi(qw[q][1]));
    }
    SBAR();
    SB(1);
#pragma unroll
    for (int q = 3; q < 5; ++q) {
      pq[q * 4] = phi_f(bf_lo(qw[q][0])); pq[q * 4 + 1] = phi_f(bf_hi(qw[q][0]));
      pq[q * 4 + 2] = phi_f(bf_lo(qw[q][1])); pq[q * 4 + 3] = phi_f(bf_hi(qw[q][1]));
    }
    *(LAS u32x4*)(uintptr_t)(scr + 8192 + st * 16) = w0;
    if (st + 256 < (b < 128 ? 400 : 480)) *(LAS u32x4*)(uintptr_t)(scr + 8192 + (st + 256) * 16) = w1;
    if (b < 128) {
      *(LAS u32x4*)(uintptr_t)(scr + st * 16) = u32x4{0, 0, 0, 0};
      *(LAS u32x4*)(uintptr_t)(scr + 4096 + st * 16) = u32x4{0, 0, 0, 0};
      if (st < 240) *(LAS u32x4*)(uintptr_t)(scr + 8192 + 6400 + st * 16) = u32x4{0, 0, 0, 0};
    }
    WAIT_LGKM0();
    SB(3);
    if (sw == 0) wait_flags(flags, 16, 16u, lane);
    SB(1);
    u32x4 tb0, tb1;
    {
      unsigned o0 = (((b < 128 ? b >> 3 : 0) * 2 + (st < 200 ? st / 100 : 0)) * 420 + (st % 100) * 4) * 4;
      int i4 = (st < 80 ? st : 0) * 4;
      unsigned o1 = (((b & 1) * 16 + i4 / 20) * 420 + 400 + i4 % 20) * 4;
      asm volatile("global_load_dwordx4 %0, %1, %2 sc1" : "=v"(tb0) : "v"(o0), "s"(p.ws + WS_KVR));
      asm volatile("global_load_dwordx4 %0, %1, %2 sc1" : "=v"(tb1) : "v"(o1), "s"(p.ws + WS_KVR));
    }
    const int q4 = fr >> 2, pp = fr & 3;
    if (b >= 128) {
#pragma unroll
      for (int it = 0; it < 2; ++it) {
        int blk = (it * 4 + sw) * 4 + g;
        int bb = blk < 30 ? blk : 0;
        int kc = bb / 5, nf = bb - kc * 5;
        unsigned wa = scr + 8192 + (kc * 8 + q4) * 160 + nf * 32 + pp * 8;
        bf16x8 v = cat8(lds_tr1(wa), lds_tr1(wa + 4 * 160));
        if (blk < 30) store16_sc1(p.ws + WS_B3, ((mcb * 176 + 80 + ((b - 128) >> 3) * 6 + kc) * 80 + nf * 16 + fr) * 16, __builtin_bit_cast(u32x4, v));
      }
    }
    SB(5);
    asm volatile("s_waitcnt vmcnt(0)" : "+v"(tb0), "+v"(tb1) :: "memory");
    if (st < 80) *(LAS u32x4*)(zt + st * 4) = tb1;
    if (b < 128 && st < 200) {
      int hh = st / 100, w = (st - hh * 100) * 4, dl = w / 20, el = w - dl * 20;
      int d = hh * 20 + dl, e = hh * 20 + el;
      int row = (d >> 5) * 32 + ((d >> 2) & 1) * 16 + ((d >> 3) & 3) * 4 + (d & 3);
      f32x4 kf = __builtin_bit_cast(f32x4, tb0);
      unsigned a = scr + row * 128 + ((((e >> 3) ^ ((row >> 1) & 7)) << 4) | ((e & 4) << 1));
      *(LAS u32x2*)(uintptr_t)a = u32x2{pack_bf16(kf[0], kf[1]), pack_bf16(kf[2], kf[3])};
    }
    WAIT_LGKM0();
    SB(1);
    {
      float den = 0.f;
#pragma unroll
      for (int q = 0; q < 5; ++q) {
        f32x4 zz = *(LAS f32x4*)(zt + (st & 15) * 20 + q * 4);
        den += pq[q * 4] * zz[0] + pq[q * 4 + 1] * zz[1] + pq[q * 4 + 2] * zz[2] + pq[q * 4 + 3] * zz[3];
      }
      float inv = 1.f / (den + 1e-6f);
#pragma unroll
      for (int q = 0; q < 5; ++q)
        qw[q] = u32x2{pack_bf16(pq[q * 4] * inv, pq[q * 4 + 1] * inv), pack_bf16(pq[q * 4 + 2] * inv, pq[q * 4 + 3] * inv)};
    }
    SBAR();
    SB(1);
#pragma unroll
    for (int it = 0; it < 3; ++it) {
      if (b < 128) {
        int unit = sw + it * 4;
        int u = unit < 10 ? unit : 0;
        int db = u / 5, nf = u - db * 5;
        f32x4 d0 = {0, 0, 0, 0}, d1 = {0, 0, 0, 0};
#pragma unroll
        for (int ks = 0; ks < 2; ++ks) {
          unsigned wa = scr + 8192 + (ks * 32 + g * 8 + q4) * 160 + nf * 32 + pp * 8;
          bf16x8 bw = cat8(lds_tr1(wa), lds_tr1(wa + 4 * 160));
          unsigned aa = scr + (db * 32 + fr) * 128 + (((ks * 4 + g) ^ ((fr >> 1) & 7)) << 4);
          d0 = mfma16(lds_b128(aa), bw, d0);
          d1 = mfma16(lds_b128(aa + 2048), bw, d1);
        }
        int kc = db * 4 + g;
        if (unit < 10 && kc < 5) {
          u32x4 o = {pack_bf16(d0[0], d0[1]), pack_bf16(d0[2], d0[3]), pack_bf16(d1[0], d1[1]), pack_bf16(d1[2], d1[3])};
          store16_sc1(p.ws + WS_B3, ((mcb * 176 + (b >> 3) * 5 + kc) * 80 + nf * 16 + fr) * 16, o);
        }
      }
      WAIT_LGKM0();
      SB(1);
    }
    constexpr int PST = 640 + 16;
    {
      unsigned a = scr + (st >> 4) * PST + (st & 15) * 40;
#pragma unroll
      for (int q = 0; q < 5; ++q) *(LAS u32x2*)(uintptr_t)(a + q * 8) = qw[q];
    }
    WAIT_LGKM0();
    SB(1);
    {
      const int prg = b >> 3, r0 = ((b >> 1) & 3) * 16;
#pragma unroll
      for (int it = 0; it < 3; ++it) {
        int idx = st + it * 256;
        if (idx < 640) {
          int ktl = idx >> 7, row = (idx >> 3) & 15, cp = idx & 7;
          int c = cp ^ (((r0 + row) >> 1) & 7);
          u32x4 v = *(LAS u32x4*)(uintptr_t)(scr + row * PST + (ktl * 8 + c) * 16);
          store16_sc1(p.ws + WS_UA, ((prg * 22 + (b & 1) * 5 + ktl) * 64 + r0 + row) * 128 + cp * 16, v);
        }
      }
    }
    SB(5);
    WAIT_VM(0);
    SB(1);
    FLAG_ADD_S(512 + (b & 15) * 32);
    SB(12);
    if (sw == 0) wait_flags(flags + 512, 16, 16u, lane);
    SB(7);
    {
      constexpr int OST = 96 * 2 + 16;
#pragma unroll
      for (int it = 0; it < 3; ++it) {
        int idx = st + it * 256;
        int row = idx / CPR, c = idx - row * CPR;
        u32x4 v = *(LAS u32x4*)(uintptr_t)(scr + row * OST + c * 16);
        int cg = cb * 12 + c;
        store16_sc1(p.ws + WS_UA, ((rg * 22 + 10 + (cg >> 3)) * 64 + row) * 128 + (((cg & 7) ^ ((row >> 1) & 7)) << 4), v);
      }
    }
    SB(2);
    WAIT_VM(0);
    SB(1);
    FLAG_ADD_S(1024 + rg * 32);
    SB(6);
    if (sw == 0) wait_flags(flags + 1024 + rg3 * 32, 1, 8u, lane);
    SB(1);
  } else if (wid >= 8) {
    const int lw = wid - 8, lt = tid - 512;
    unsigned aoff1, aoff2, boff1[3];
    {
      int row = lw * 8 + (lane >> 3), c = (lane & 7) ^ ((row >> 1) & 7);
      aoff1 = ((rg * 64 + row) * D_ + c * 8) * 2;
      aoff2 = ((rg * 64 + row) * S_ + c * 8) * 2;
    }
#pragma unroll
    for (int i = 0; i < 3; ++i) {
      int slot = i * 256 + lt;
      int k = slot / CPR;
      boff1[i] = (k * (2 * F_) + cb * 96 + bswz<false>(k, slot - k * CPR) * 8) * 2;
    }
    const unsigned boff2 = lt * 16;
    const u16* zpanel = (const u16*)(p.ws + WS_Z2N) + (size_t)cb * S_ * 96;
    const u16* bpanel = (const u16*)(p.ws + WS_B3) + (size_t)cb3 * KA_ * 80;
    const u16* ua = (const u16*)(p.ws + WS_UA) + (size_t)rg3 * 22 * 4096;
    const bool b3 = lw < 2;
    const unsigned lbase = lds0 + lw * 1024;
    auto ld_z1_a = [&](int t, int sl) {
#pragma unroll
      for (int i = 0; i < 2; ++i) glds16(p.x + t * 64 + i * 32 * D_, aoff1, lbase + sl * K2_SLOT + i * 4096);
    };
    auto ld_z1_b = [&](int t, int sl) {
#pragma unroll
      for (int i = 0; i < 3; ++i) glds16(p.w_in + (size_t)t * 64 * (2 * F_), boff1[i], lbase + sl * K2_SLOT + 8192 + i * 4096);
    };
    auto ld_g_a = [&](int t, int sl) {
#pragma unroll
      for (int i = 0; i < 2; ++i) glds16(p.w_sp + t * 64 + i * 32 * S_, aoff2, lbase + sl * K2_SLOT + i * 4096);
    };
    auto ld_g_b = [&](int t, int sl) {
#pragma unroll
      for (int i = 0; i < 3; ++i) glds16_sc1(zpanel + (size_t)t * 64 * 96 + i * 2048, boff2, lbase + sl * K2_SLOT + 8192 + i * 4096);
    };
    auto ld_o_a = [&](int t, int sl) {
#pragma unroll
      for (int i = 0; i < 2; ++i) glds16_sc1(ua + t * 4096 + i * 2048, boff2, lbase + sl * K2_SLOT + i * 4096);
    };
    auto ld_o_b = [&](int t, int sl) {
#pragma unroll
      for (int i = 0; i < 2; ++i) glds16_sc1(bpanel + (size_t)t * 64 * 80 + i * 2048, boff2, lbase + sl * K2_SLOT + 8192 + i * 4096);
      if (b3) glds16_sc1(bpanel + (size_t)t * 64 * 80 + 4096, boff2, lbase + sl * K2_SLOT + 8192 + 8192);
    };
    int sl = 0;
    auto nx = [&]() { sl = sl == K2_NS - 1 ? 0 : sl + 1; };
#pragma unroll 1
    for (int t = 0; t < 2; ++t) { ld_z1_a(t, sl); ld_z1_b(t, sl); nx(); }
    WAIT_VM(5); BARRIER();
#pragma unroll 1
    for (int t = 2; t < 4; ++t) { ld_z1_a(t, sl); ld_z1_b(t, sl); nx(); }
    WAIT_VM(10); BARRIER();
#pragma unroll 1
    for (int t = 4; t < 6; ++t) { ld_z1_a(t, sl); ld_z1_b(t, sl); nx(); }
#pragma unroll 1
    for (int t = 6; t < 10; ++t) { ld_z1_a(t, sl); ld_z1_b(t, sl); nx(); WAIT_VM(20); BARRIER(); }
    ld_g_a(0, 3); WAIT_VM(17); BARRIER();
    ld_g_a(1, 4); WAIT_VM(14); BARRIER();
    ld_g_b(0, 3); ld_g_b(1, 4);
    sl = 5;
#pragma unroll 1
    for (int t = 2; t < 32; ++t) {
      ld_g_a(t, sl); ld_g_b(t, sl); nx();
      if (t == 4) WAIT_VM(18); else WAIT_VM(20);
      BARRIER();
    }
#pragma unroll 1
    for (int t = 0; t < 10; ++t) {
      ld_o_a(t, sl); ld_o_b(t, sl); nx();
      if (b3) WAIT_VM(20); else WAIT_VM(16);
      BARRIER();
    }
    const int sl52 = sl;
    ld_o_b(10, sl); nx(); if (b3) WAIT_VM(18); else WAIT_VM(14); BARRIER();
    ld_o_b(11, sl); nx(); if (b3) WAIT_VM(16); else WAIT_VM(12); BARRIER();
    ld_o_b(12, sl); nx(); if (b3) WAIT_VM(14); else WAIT_VM(10); BARRIER();
    ld_o_b(13, sl); nx(); if (b3) WAIT_VM(12); else WAIT_VM(8); BARRIER();
    ld_o_b(14, sl); nx(); BARRIER();
    ld_o_b(15, sl); nx(); BARRIER();
    {
      int s2 = sl52;
#pragma unroll 1
      for (int t = 10; t < 16; ++t) { ld_o_a(t, s2); s2 = s2 == K2_NS - 1 ? 0 : s2 + 1; }
    }
    WAIT_VM(10); BARRIER();
    WAIT_VM(8); BARRIER();
#pragma unroll 1
    for (int t = 16; t < 22; ++t) {
      ld_o_a(t, sl); ld_o_b(t, sl); nx();
      if (t == 16) WAIT_VM(10); else if (t == 17) WAIT_VM(12); else if (t == 18) WAIT_VM(14); else WAIT_VM(16);
      BARRIER();
    }
    if (b3) WAIT_VM(15); else WAIT_VM(12); BARRIER();
    if (b3) WAIT_VM(10); else WAIT_VM(8); BARRIER();
    if (b3) WAIT_VM(5); else WAIT_VM(4); BARRIER();
    WAIT_VM(0); BARRIER();
    BARRIER();
    BARRIER();
  } else {
    const int wr = wid >> 1, wc = wid & 1;
    const int kh = wid >> 2, wm = wid & 3;
    const int mcb = b & 7;
    const unsigned scr = lds0 + K2_SCR;
    LAS float* sx = (LAS float*)(uintptr_t)scr;
    LAS float* cst = (LAS float*)(uintptr_t)(scr + 16384);
    LAS float* krp = (LAS float*)(uintptr_t)(scr + 16384);
    const int urow = wr * 16 + fr;

    constexpr int ZST = 1536 + 32;
    u32x2 yv[3];
    u32x4 tb0;
    {
      unsigned oy = ((b * 8 + wid) * F_ + lane * 4) * 2;
#pragma unroll
      for (int i = 0; i < 3; ++i) asm volatile("global_load_dwordx2 %0, %1, %2" : "=v"(yv[i]) : "v"(oy + i * 512), "s"(p.ws + WS_Y2));
      unsigned ok = (((tid >> 4) & 15) * 13440 + (b < 210 ? b : 0) * 64 + (tid & 15) * 4) * 4;
      asm volatile("global_load_dwordx4 %0, %1, %2" : "=v"(tb0) : "v"(ok), "s"(p.ws + WS_KVP));
    }
    auto parta_compute = [&]() {
      asm volatile("s_waitcnt vmcnt(0)" : "+v"(yv[0]), "+v"(yv[1]), "+v"(yv[2]), "+v"(tb0) :: "memory");
      float z[12];
#pragma unroll
      for (int i = 0; i < 3; ++i)
#pragma unroll
        for (int q = 0; q < 2; ++q) {
          unsigned w = pack_bf16(gelu_f(bf_lo(yv[i][q])), gelu_f(bf_hi(yv[i][q])));
          z[i * 4 + q * 2] = bf_lo(w);
          z[i * 4 + q * 2 + 1] = bf_hi(w);
        }
      float s = 0.f;
#pragma unroll
      for (int i = 0; i < 12; ++i) s += z[i];
      s = wave_sum(s);
      float mean = s * (1.f / F_), v2 = 0.f;
#pragma unroll
      for (int i = 0; i < 12; ++i) { z[i] -= mean; v2 = __builtin_fmaf(z[i], z[i], v2); }
      v2 = wave_sum(v2);
      float rstd = __builtin_amdgcn_rsqf(v2 * (1.f / F_) + 1e-5f);
#pragma unroll
      for (int i = 0; i < 3; ++i)
        *(LAS u32x2*)(uintptr_t)(scr + wid * ZST + (i * 256 + lane * 4) * 2) =
            u32x2{pack_bf16(z[i * 4] * rstd, z[i * 4 + 1] * rstd), pack_bf16(z[i * 4 + 2] * rstd, z[i * 4 + 3] * rstd)};
      if (wid < 4) *(LAS u32x4*)(krp + tid * 4) = tb0;
      WAIT_LGKM0();
    };
    auto parta_store = [&]() {
#pragma unroll
      for (int it = 0; it < 2; ++it) {
        int grp64 = it ? 8 + (wid & 3) : wid;
        int q = fr >> 2, pp = fr & 3;
        unsigned a = scr + q * ZST + (grp64 * 64 + g * 16 + pp * 4) * 2;
        bf16x8 v = cat8(lds_tr1(a), lds_tr1(a + 4 * ZST));
        int f = grp64 * 64 + lane, c = f / 96, n = f - c * 96;
        if (it == 0 || wid < 4) store16_sc1(p.ws + WS_Z2N, ((c * 256 + b) * 96 + n) * 16, __builtin_bit_cast(u32x4, v));
      }
      if (tid >= 448 && tid < 464 && b < 210) {
        int v = tid - 448;
        f32x4 t4 = {0, 0, 0, 0};
#pragma unroll
        for (int q = 0; q < 16; ++q) t4 += *(LAS f32x4*)(krp + q * 64 + v * 4);
        store16_sc1(p.ws + WS_KVR, (b * 64 + v * 4) * 4, __builtin_bit_cast(u32x4, t4));
      }
    };

    unsigned abase[2], baddr1[3], baddr2, abase3, baddr3;
    {
      int x7 = (fr >> 1) & 7;
#pragma unroll
      for (int ks = 0; ks < 2; ++ks) abase[ks] = lds0 + (wr * 16 + fr) * 128 + (((ks * 4 + g) ^ x7) << 4);
      int q = fr >> 2, pp = fr & 3;
#pragma unroll
      for (int n = 0; n < 3; ++n) {
        int k = 8 * g + q;
        baddr1[n] = lds0 + 8192 + (k * CPR + bswz<false>(k, 2 * (wc * 3 + n) + (pp >> 1))) * 16 + (pp & 1) * 8;
      }
      baddr2 = lds0 + 8192 + (g * 96 + wc * 48 + fr) * 16;
      abase3 = lds0 + (wm * 16 + fr) * 128 + (((kh * 4 + g) ^ x7) << 4);
      baddr3 = lds0 + 8192 + ((kh * 4 + g) * 80 + fr) * 16;
    }
    f32x4 acc1[3], acc2[3], cs[3], acc3[5];
#pragma unroll
    for (int n = 0; n < 3; ++n) { acc1[n] = f32x4{0, 0, 0, 0}; acc2[n] = f32x4{0, 0, 0, 0}; cs[n] = f32x4{0, 0, 0, 0}; }
#pragma unroll
    for (int n = 0; n < 5; ++n) acc3[n] = f32x4{0, 0, 0, 0};
    const short one = 0x3f80;
    bf16x8 ones = {one, one, one, one, one, one, one, one};
    asm volatile("" : "+v"(ones));

    bf16x8 fA[8], fB[8];
    unsigned slot = 0;
    int z1t = 0;
    auto adv = [&]() { slot = slot == (K2_NS - 1) * K2_SLOT ? 0 : slot + K2_SLOT; };
    auto ldf = [&](bf16x8 (&f)[8], int mode, int i, unsigned& tmp) {
      if (mode == 2) {
        if (i == 0) f[0] = lds_b128(pinv(abase3 + slot));
        else if (i < 6) { if (i == 1) tmp = pinv(baddr3 + slot); f[1 + i] = lds_b128(tmp + (i - 1) * 256); }
      } else {
        if (i == 0 || i == 4) f[i >> 2] = lds_b128(pinv(abase[i >> 2] + slot));
        else {
          int ks = i >> 2, n = (i & 3) - 1;
          if (mode == 0) {
            unsigned a = pinv(baddr1[n] + slot) + ks * 32 * CPR * 16;
            f[2 + ks * 3 + n] = cat8(lds_tr1(a), lds_tr1(a + 4 * CPR * 16));
          } else {
            if (i == 1) tmp = pinv(baddr2 + slot);
            f[2 + ks * 3 + n] = lds_b128(tmp + ks * 4 * 96 * 16 + n * 256);
          }
        }
      }
    };
    auto iter = [&](bf16x8 (&fc)[8], bf16x8 (&fn)[8], auto mc, auto mn) {
      constexpr int MC = decltype(mc)::value, MN = decltype(mn)::value;
      unsigned tmp = 0;
#pragma unroll
      for (int i = 0; i < 8; ++i) {
        if (MC == 2) {
          if (i < 5) acc3[i] = mfma16(fc[2 + i], fc[0], acc3[i]);
        } else if (i < 6) {
          int ks = i / 3, n = i % 3;
          if (MC == 0) acc1[n] = mfma16(fc[2 + i], fc[ks], acc1[n]);
          else acc2[n] = mfma16(fc[2 + i], fc[ks], acc2[n]);
        }
        if (MN < 3) ldf(fn, MN, i, tmp);
        SBAR();
      }
      if (MC == 0) {
        if (wr == (z1t & 3)) {
#pragma unroll
          for (int i = 0; i < 6; ++i) cs[i % 3] = mfma16(fc[2 + i], ones, cs[i % 3]);
        }
        ++z1t;
        SBAR();
      }
      if (MN < 3) adv();
    };
    using I0 = std::integral_constant<int, 0>;
    using I1 = std::integral_constant<int, 1>;
    using I2 = std::integral_constant<int, 2>;
    using I3 = std::integral_constant<int, 3>;
#define FLAG_ADD(off) do { if (tid == 0) __hip_atomic_fetch_add((gu32*)(flags + (off)), 1u, __ATOMIC_RELAXED, __HIP_MEMORY_SCOPE_AGENT); } while (0)
    const f32x2 xs = *(const f32x2*)((const float*)(p.ws + WS_XST) + (size_t)(rg * 64 + urow) * 2);
    const float bias = bf_lo(p.b_sp[rg * 64 + urow]);
    parta_compute();
    STAMP(1, 209);
    BARRIER();
    STAMP(1, 0);
    parta_store();
    {
      unsigned tmp = 0;
#pragma unroll
      for (int i = 0; i < 8; ++i) ldf(fA, 0, i, tmp);
      adv();
    }
    BARRIER();
    iter(fA, fB, I0{}, I0{}); WAIT_VM(0); BARRIER();
    FLAG_ADD((b & 15) * 32);
    STAMP(1, 1);
    iter(fB, fA, I0{}, I0{}); BARRIER();
    iter(fA, fB, I0{}, I0{}); BARRIER();
    iter(fB, fA, I0{}, I0{}); BARRIER();
    STAMP(1, 4);
    iter(fA, fB, I0{}, I0{}); BARRIER();
    iter(fB, fA, I0{}, I0{}); BARRIER();
    STAMP(1, 6);
    iter(fA, fB, I0{}, I0{}); BARRIER();
    iter(fB, fA, I0{}, I0{}); BARRIER();
    iter(fA, fB, I0{}, I0{}); BARRIER();
    iter(fB, fA, I0{}, I1{}); BARRIER();
    STAMP(1, 10);
#pragma unroll 1
    for (int t = 10; t < 18; t += 2) {
      iter(fA, fB, I1{}, I1{}); BARRIER();
      iter(fB, fA, I1{}, I1{}); BARRIER();
    }
    STAMP(1, 18);
    iter(fA, fB, I1{}, I1{});
    if (fr == 0) {
#pragma unroll
      for (int n = 0; n < 3; ++n) *(LAS f32x4*)(cst + wr * 96 + wc * 48 + n * 16 + g * 4) = cs[n];
    }
    WAIT_LGKM0();
    BARRIER();
    auto z1act = [&](int n) {
      SBAR();
      int c0 = wc * 48 + n * 16 + g * 4;
      f32x4 c4 = (*(LAS f32x4*)(cst + c0) + *(LAS f32x4*)(cst + 96 + c0)) + (*(LAS f32x4*)(cst + 192 + c0) + *(LAS f32x4*)(cst + 288 + c0));
#pragma unroll
      for (int r = 0; r < 4; ++r) acc1[n][r] = gelu_f(__builtin_fmaf(acc1[n][r], xs[1], c4[r] * xs[0]));
      asm volatile("" : "+v"(acc1[n]));
      SBAR();
    };
    iter(fB, fA, I1{}, I1{}); z1act(0); BARRIER();
    iter(fA, fB, I1{}, I1{}); z1act(1); BARRIER();
    iter(fB, fA, I1{}, I1{}); z1act(2); BARRIER();
    iter(fA, fB, I1{}, I1{}); BARRIER();
    u32x4 xv0, xv1;
    {
      int i0 = tid, i1 = tid < 128 ? tid + 512 : tid;
      xv0 = *(const u32x4*)(p.x + (size_t)(rg3 * 64 + i0 / 10) * D_ + cb3 * 80 + (i0 % 10) * 8);
      xv1 = *(const u32x4*)(p.x + (size_t)(rg3 * 64 + i1 / 10) * D_ + cb3 * 80 + (i1 % 10) * 8);
    }
    iter(fB, fA, I1{}, I1{}); BARRIER();
#pragma unroll 1
    for (int t = 24; t < 34; t += 2) {
      iter(fA, fB, I1{}, I1{}); BARRIER();
      iter(fB, fA, I1{}, I1{}); BARRIER();
    }
    iter(fA, fB, I1{}, I1{}); BARRIER();
    iter(fB, fA, I1{}, I1{}); BARRIER();
#pragma unroll 1
    for (int t = 36; t < 40; t += 2) {
      iter(fA, fB, I1{}, I1{}); BARRIER();
      iter(fB, fA, I1{}, I1{}); BARRIER();
    }
    iter(fA, fB, I1{}, I1{}); BARRIER();
    iter(fB, fA, I1{}, I2{});
    STAMP(1, 42);
    constexpr int OST = 96 * 2 + 16;
    {
      unsigned dst = scr + urow * OST + (wc * 48 + g * 4) * 2;
#pragma unroll
      for (int n = 0; n < 3; ++n) {
        float v[4];
#pragma unroll
        for (int r = 0; r < 4; ++r) v[r] = acc1[n][r] * (acc2[n][r] + bias);
        *(LAS u32x2*)(uintptr_t)(dst + n * 32) = u32x2{pack_bf16(v[0], v[1]), pack_bf16(v[2], v[3])};
      }
    }
    SBAR();
    WAIT_LGKM0();
    BARRIER();
    iter(fA, fB, I2{}, I2{}); BARRIER();
    STAMP(1, 43);
    iter(fB, fA, I2{}, I2{}); BARRIER();
    iter(fA, fB, I2{}, I2{}); BARRIER();
    iter(fB, fA, I2{}, I2{}); BARRIER();
    STAMP(1, 46);
#pragma unroll 1
    for (int t = 46; t < 50; t += 2) {
      iter(fA, fB, I2{}, I2{}); BARRIER();
      iter(fB, fA, I2{}, I2{}); BARRIER();
    }
    iter(fA, fB, I2{}, I2{}); BARRIER();
    STAMP(1, 51);
    iter(fB, fA, I2{}, I3{});
    BARRIER();
    STAMP(1, 52);
    BARRIER();
    {
      unsigned tmp = 0;
#pragma unroll
      for (int i = 0; i < 8; ++i) ldf(fA, 2, i, tmp);
      adv();
    }
    BARRIER();
    STAMP(1, 53);
#pragma unroll 1
    for (int t = T_B; t < T_END; t += 2) {
      iter(fA, fB, I2{}, I2{}); BARRIER();
      iter(fB, fA, I2{}, I2{});
      if (t == T_END - 2) WAIT_LGKM0();
      BARRIER();
    }
    STAMP(1, 100);
    constexpr int OS3 = 80 * 4 + 16;
    {
      unsigned dst = lds0 + kh * (64 * OS3) + (wm * 16 + fr) * OS3 + g * 16;
#pragma unroll
      for (int n = 0; n < 5; ++n) *(LAS f32x4*)(uintptr_t)(dst + n * 64) = acc3[n];
    }
    WAIT_LGKM0();
    BARRIER();
#pragma unroll
    for (int it = 0; it < 2; ++it) {
      int idx = tid + it * 512;
      if (idx < 640) {
        int row = idx / 10, c = idx - row * 10;
        u32x4 xv = it ? xv1 : xv0;
        unsigned la = lds0 + row * OS3 + c * 32;
        f32x4 a = *(LAS f32x4*)(uintptr_t)(la) + *(LAS f32x4*)(uintptr_t)(la + 64 * OS3);
        f32x4 c4 = *(LAS f32x4*)(uintptr_t)(la + 16) + *(LAS f32x4*)(uintptr_t)(la + 64 * OS3 + 16);
        u32x4 o = {pack_bf16(bf_lo(xv[0]) + a[0], bf_hi(xv[0]) + a[1]), pack_bf16(bf_lo(xv[1]) + a[2], bf_hi(xv[1]) + a[3]),
                   pack_bf16(bf_lo(xv[2]) + c4[0], bf_hi(xv[2]) + c4[1]), pack_bf16(bf_lo(xv[3]) + c4[2], bf_hi(xv[3]) + c4[3])};
        *(u32x4*)(p.out + (size_t)(rg3 * 64 + row) * D_ + cb3 * 80 + c * 8) = o;
      }
    }
    STAMP(1, 201);
  }
}

extern "C" void kernel_launch(void* const* d_in, const int* in_sizes, int n_in, void* d_out, int out_size, void* d_ws,
                              size_t ws_size, hipStream_t stream) {
  Params p;
  p.x = (const u16*)d_in[0];
  p.wqkv = (const u16*)d_in[1];
  p.wo = (const u16*)d_in[2];
  p.w_in = (const u16*)d_in[4];
  p.w_sp = (const u16*)d_in[5];
  p.b_sp = (const u16*)d_in[6];
  p.w_out = (const u16*)d_in[7];
  p.out = (u16*)d_out;
  p.ws = (char*)d_ws;
  (void)hipFuncSetAttribute((const void*)stage1_kernel, hipFuncAttributeMaxDynamicSharedMemorySize, S1_LDS);
  (void)hipFuncSetAttribute((const void*)stage2_kernel, hipFuncAttributeMaxDynamicSharedMemorySize, K2_LDS);
  stage1_kernel<<<256, S1_THREADS, S1_LDS, stream>>>(p);
  stage2_kernel<<<256, K2_THREADS, K2_LDS, stream>>>(p);
}

#if defined(__HIP_DEVICE_COMPILE__) && __HIP_DEVICE_COMPILE__
#pragma clang attribute pop
#endif
```

```cpp
#if defined(__HIP_DEVICE_COMPILE__) && __HIP_DEVICE_COMPILE__
#pragma clang attribute push(__attribute__((target("no-packed-fp32-ops"))), apply_to = function)
#endif
#include <hip/hip_runtime.h>
#include <hip/hip_bf16.h>
#include <stdint.h>
#include <type_traits>

typedef short bf16x8 __attribute__((ext_vector_type(8)));
typedef short s16x4 __attribute__((ext_vector_type(4)));
typedef float f32x4 __attribute__((ext_vector_type(4)));
typedef float f32x2 __attribute__((ext_vector_type(2)));
typedef unsigned u32x4 __attribute__((ext_vector_type(4)));
typedef unsigned u32x2 __attribute__((ext_vector_type(2)));
typedef __bf16 bf16x2_t __attribute__((ext_vector_type(2)));
typedef unsigned short u16;

#define TIDX ((int)__builtin_amdgcn_workitem_id_x())
#define BIDX ((int)__builtin_amdgcn_workgroup_id_x())
#define LAS __attribute__((address_space(3)))
#define DI __device__ __forceinline__

constexpr int S_ = 2048, D_ = 640, H_ = 32, DH_ = 20, F_ = 768, KA_ = D_ + F_;

constexpr size_t WS_Y2 = 0;
constexpr size_t WS_Q = WS_Y2 + (size_t)S_ * F_ * 2;
constexpr size_t WS_UA = WS_Q + (size_t)S_ * D_ * 2;
constexpr size_t WS_B3 = WS_UA + (size_t)S_ * KA_ * 2;
constexpr size_t WS_KVP = WS_B3 + (size_t)KA_ * D_ * 2;
constexpr size_t WS_XST = WS_KVP + (size_t)16 * 32 * 420 * 4;
constexpr size_t WS_Z2N = WS_XST + (size_t)S_ * 2 * 4;
constexpr size_t WS_END = WS_Z2N + (size_t)S_ * F_ * 2;
constexpr size_t WS_FLAGS = WS_END;
constexpr size_t WS_KVR = WS_FLAGS + 64 * 32 * 4;

DI unsigned pack_bf16(float a, float b) {
  f32x2 v = {a, b};
  bf16x2_t r = __builtin_convertvector(v, bf16x2_t);
  return __builtin_bit_cast(unsigned, r);
}
DI float bf_lo(unsigned w) { return __builtin_bit_cast(float, w << 16); }
DI float bf_hi(unsigned w) { return __builtin_bit_cast(float, w & 0xffff0000u); }
DI float phi_f(float t) {
  return (fmaxf(t, 0.f) + 1e-6f) + __builtin_amdgcn_exp2f(fminf(t, 0.f) * 1.4426950408889634f);
}
DI float gelu_f(float x) {
  constexpr float c1 = -2.f * 0.7978845608028654f * 1.4426950408889634f;
  constexpr float c3 = c1 * 0.044715f;
  float a = x * __builtin_fmaf(x * x, c3, c1);
  return x * __builtin_amdgcn_rcpf(1.f + __builtin_amdgcn_exp2f(a));
}
DI float wave_sum(float v) {
  v += __builtin_bit_cast(float, __builtin_amdgcn_update_dpp(0, __builtin_bit_cast(int, v), 0xB1, 0xf, 0xf, false));
  v += __builtin_bit_cast(float, __builtin_amdgcn_update_dpp(0, __builtin_bit_cast(int, v), 0x4E, 0xf, 0xf, false));
  v += __builtin_bit_cast(float, __builtin_amdgcn_update_dpp(0, __builtin_bit_cast(int, v), 0x141, 0xf, 0xf, false));
  v += __builtin_bit_cast(float, __builtin_amdgcn_update_dpp(0, __builtin_bit_cast(int, v), 0x140, 0xf, 0xf, false));
  int iv = __builtin_bit_cast(int, v);
  float r0 = __builtin_bit_cast(float, __builtin_amdgcn_readlane(iv, 0)), r1 = __builtin_bit_cast(float, __builtin_amdgcn_readlane(iv, 16));
  float r2 = __builtin_bit_cast(float, __builtin_amdgcn_readlane(iv, 32)), r3 = __builtin_bit_cast(float, __builtin_amdgcn_readlane(iv, 48));
  return (r0 + r1) + (r2 + r3);
}
DI f32x4 mfma16(bf16x8 a, bf16x8 b, f32x4 c) { return __builtin_amdgcn_mfma_f32_16x16x32_bf16(a, b, c, 0, 0, 0); }
DI bf16x8 lds_b128(unsigned addr) { return *(LAS bf16x8*)(uintptr_t)addr; }
DI s16x4 lds_tr1(unsigned a) { return __builtin_amdgcn_ds_read_tr16_b64_v4i16((LAS s16x4*)(uintptr_t)a); }
DI bf16x8 cat8(s16x4 lo, s16x4 hi) { return __builtin_shufflevector(lo, hi, 0, 1, 2, 3, 4, 5, 6, 7); }
DI void glds16(const void* sbase, unsigned voff, unsigned lds_addr) {
  unsigned sv;
  asm volatile("s_mov_b32 %0, m0\n\ts_mov_b32 m0, %3\n\ts_nop 0\n\tglobal_load_lds_dwordx4 %1, %2\n\ts_mov_b32 m0, %0"
               : "=&s"(sv) : "v"(voff), "s"(sbase), "s"(lds_addr));
}
DI void touch_line(unsigned& sink, const void* sbase, unsigned voff) {
  asm volatile("global_load_dword %0, %1, %2" : "=v"(sink) : "v"(voff), "s"(sbase));
}
#define WAIT_VM(n) asm volatile("s_waitcnt vmcnt(%0)" ::"i"(n) : "memory")
#define WAIT_LGKM0() asm volatile("s_waitcnt lgkmcnt(0)" ::: "memory")
#define BARRIER() do { asm volatile("" ::: "memory"); __builtin_amdgcn_s_barrier(); asm volatile("" ::: "memory"); } while (0)
#define SBAR() __builtin_amdgcn_sched_barrier(0)
DI unsigned pinv(unsigned a) { asm volatile("" : "+v"(a)); return a; }

#define STAMP(kid, idx)

struct Params {
  const u16* x;
  const u16* wqkv;
  const u16* wo;
  const u16* w_in;
  const u16* w_sp;
  const u16* b_sp;
  const u16* w_out;
  u16* out;
  char* ws;
};

template <bool XQ>
DI int bswz(int k, int c) { return c ^ (((k >> 3) & 1) << 1) ^ (XQ ? (((k >> 1) & 1) << 2) : 0); }

constexpr int S1_STAGE = 16384 + 24576;
constexpr int S1_NBUF = 4;
constexpr int S1_LDS = S1_NBUF * S1_STAGE;
constexpr int S1_THREADS = 768;

template <int TYPE>
DI void stage1_body(const Params& p, int rg, int ct, unsigned lds0, int xcd, int jb) {
  constexpr int BN = TYPE == 2 ? 192 : 160;
  constexpr int CPR = BN / 8;
  constexpr int NF = BN / 32;
  constexpr int BI = BN / 32;
  constexpr int G = 4 + BI;
  constexpr int NT = D_ / 64;
  constexpr bool XQ = TYPE == 2;
  const int tid = TIDX, lane = tid & 63;
  const int wid = __builtin_amdgcn_readfirstlane(tid >> 6);
  const int fr = lane & 15, g = lane >> 4;
  const int wr = (wid >> 1) & 3, wc = wid & 1;

  LAS float* st_rs = (LAS float*)(uintptr_t)lds0;
  LAS float* st_sq = st_rs + 128;
  LAS float* st_cs = (LAS float*)(uintptr_t)(lds0 + 1024);
  const unsigned otile = lds0 + 4096;
  constexpr int OST = BN * 2 + 16;

  if (wid >= 8) {
    const int lw = wid - 8;
    const u16* Bm = TYPE == 2 ? p.w_in : p.wqkv;
    constexpr int ldb = TYPE == 2 ? 2 * F_ : 3 * D_;
    unsigned aoff, boff[BI];
    {
      int row = lw * 8 + (lane >> 3), c = (lane & 7) ^ ((row >> 1) & 7);
      aoff = ((rg * 128 + row) * D_ + c * 8) * 2;
    }
#pragma unroll
    for (int i = 0; i < BI; ++i) {
      int slot = (i * 4 + lw) * 64 + lane;
      int k = slot / CPR, c = bswz<XQ>(k, slot - k * CPR);
      int col;
      if (TYPE == 0) col = c < 10 ? (D_ + ct * 80 + c * 8) : (2 * D_ + ct * 80 + (c - 10) * 8);
      else if (TYPE == 1) col = ct * 160 + c * 8;
      else col = F_ + ct * 192 + c * 8;
      boff[i] = (k * ldb + col) * 2;
    }
    auto issue = [&](int t) {
      unsigned base = lds0 + (t & 3) * S1_STAGE;
#pragma unroll
      for (int i = 0; i < 4; ++i) glds16(p.x + t * 64 + i * 32 * D_, aoff, base + (i * 4 + lw) * 1024);
#pragma unroll
      for (int i = 0; i < BI; ++i) glds16(Bm + (size_t)t * 64 * ldb, boff[i], base + 16384 + (i * 4 + lw) * 1024);
    };
    issue(0);
    issue(1);
    unsigned sk[5];
    WAIT_VM(G);
    BARRIER();
    issue(2);
    WAIT_VM(G);
    BARRIER();
#pragma unroll 1
    for (int t = 0; t < NT - 4; ++t) {
      issue(t + 3);
      WAIT_VM(G);
      BARRIER();
    }
    issue(NT - 1);
    WAIT_VM(G);
    BARRIER();
    WAIT_VM(0);
    BARRIER();
    if (TYPE != 0) {
      const int lt = lw * 64 + lane;
      const int i16 = (TYPE - 1) * 8 + (jb & 7);
      const int i128 = xcd * 16 + i16;
      touch_line(sk[0], p.w_sp, ((((xcd & 3) * 2 + (xcd >> 2)) * 16 + i16) * 512 + lt) * 128);
      touch_line(sk[1], p.w_sp, ((((xcd & 3) * 2 + (xcd >> 2)) * 16 + i16) * 512 + 256 + lt) * 128);
      int i = lt < 60 ? lt : 0;
      touch_line(sk[2], p.w_in, (((xcd & 3) * 160 + i16 * 10 + i / 6) * (2 * F_) + (xcd >> 2) * 384) * 2 + (i % 6) * 128);
      touch_line(sk[3], p.w_out, (i128 * 60 + (lt < 60 ? lt : 0)) * 128);
      touch_line(sk[4], p.wo, (i128 * 50 + (lt < 50 ? lt : 0)) * 128);
    }
    BARRIER();
    BARRIER();
    BARRIER();
    BARRIER();
    if (TYPE != 0) asm volatile("s_waitcnt vmcnt(0)" : "+v"(sk[0]), "+v"(sk[1]), "+v"(sk[2]), "+v"(sk[3]), "+v"(sk[4]) :: "memory");
    return;
  } else {
    unsigned abase[2], baddr[NF];
    {
      int x7 = (fr >> 1) & 7;
#pragma unroll
      for (int ks = 0; ks < 2; ++ks) abase[ks] = lds0 + (wr * 32 + wc * 16 + fr) * 128 + (((ks * 4 + g) ^ x7) << 4);
      int q = fr >> 2, pp = fr & 3;
#pragma unroll
      for (int n = 0; n < NF; ++n) {
        int c;
        if (TYPE == 0) {
          int w = 2 * n + (pp >> 1);
          c = w < 5 ? wc * 5 + w : 10 + wc * 5 + (w - 5);
        } else {
          c = 2 * (wc * NF + n) + (pp >> 1);
        }
        int k = 8 * g + q;
        baddr[n] = lds0 + 16384 + (k * CPR + bswz<XQ>(k, c)) * 16 + (pp & 1) * 8;
      }
    }
    const int am1 = wc ? -2048 : 2048;
    f32x4 acc[2][NF], cs[NF], rs, sq;
#pragma unroll
    for (int m = 0; m < 2; ++m)
#pragma unroll
      for (int n = 0; n < NF; ++n) acc[m][n] = f32x4{0, 0, 0, 0};
#pragma unroll
    for (int n = 0; n < NF; ++n) cs[n] = f32x4{0, 0, 0, 0};
    rs = f32x4{0, 0, 0, 0};
    sq = f32x4{0, 0, 0, 0};
    const short one = 0x3f80;
    bf16x8 ones = {one, one, one, one, one, one, one, one};
    asm volatile("" : "+v"(ones));

    bf16x8 af0[2], af1[2];
    s16x4 bl0[NF], bh0[NF], bl1[NF], bh1[NF];
    auto phase = [&](bf16x8 (&afc)[2], s16x4 (&blc)[NF], s16x4 (&bhc)[NF], bf16x8 (&afn)[2], s16x4 (&bln)[NF], s16x4 (&bhn)[NF],
                     unsigned sbn, int ksn, bool docs) {
      const unsigned a0 = pinv(abase[ksn] + sbn);
      unsigned ba = 0;
#pragma unroll
      for (int i = 0; i < 2 * NF + 2; ++i) {
        if (i < 2 * NF) {
          int m = i / NF, n = i % NF;
          bf16x8 bq = cat8(blc[n], bhc[n]);
          acc[m][n] = TYPE == 0 ? mfma16(afc[m], bq, acc[m][n]) : mfma16(bq, afc[m], acc[m][n]);
        } else if (i == 2 * NF) rs = mfma16(afc[0], ones, rs);
        else sq = mfma16(afc[0], afc[0], sq);
        if (i == 0) afn[0] = lds_b128(a0);
        else if (i <= 2 * NF) {
          int r = i - 1;
          if (r & 1) bhn[r >> 1] = lds_tr1(ba + (ksn * 32 + 4) * CPR * 16);
          else { ba = pinv(baddr[r >> 1] + sbn); bln[r >> 1] = lds_tr1(ba + ksn * 32 * CPR * 16); }
        } else afn[1] = lds_b128(a0 + am1);
        SBAR();
      }
      if (docs) {
#pragma unroll
        for (int n = 0; n < NF; ++n) cs[n] = mfma16(ones, cat8(blc[n], bhc[n]), cs[n]);
      }
      SBAR();
    };
    STAMP(0, 0);
    BARRIER();
    af0[0] = lds_b128(abase[0]);
    af0[1] = lds_b128(abase[0] + am1);
#pragma unroll
    for (int n = 0; n < NF; ++n) { bl0[n] = lds_tr1(baddr[n]); bh0[n] = lds_tr1(baddr[n] + 4 * CPR * 16); }
    phase(af0, bl0, bh0, af1, bl1, bh1, 0, 1, wr == 0);
    BARRIER();
    phase(af1, bl1, bh1, af0, bl0, bh0, S1_STAGE, 0, wr == 1);
    BARRIER();
#pragma unroll 1
    for (int t = 1; t < NT; ++t) {
      STAMP(0, 1 + t);
      phase(af0, bl0, bh0, af1, bl1, bh1, (t & 3) * S1_STAGE, 1, wr == ((2 * t) & 3));
      phase(af1, bl1, bh1, af0, bl0, bh0, ((t + 1) & 3) * S1_STAGE, 0, wr == ((2 * t + 1) & 3));
      if (t == NT - 1) WAIT_LGKM0();
      BARRIER();
    }
    STAMP(0, 100);
    {
      int r0 = wr * 32 + wc * 16;
      if (fr == 0) *(LAS f32x4*)(st_rs + r0 + g * 4) = rs;
      if ((fr >> 2) == g) {
        int r = fr & 3;
        st_sq[r0 + fr] = r == 0 ? sq[0] : r == 1 ? sq[1] : r == 2 ? sq[2] : sq[3];
      }
      if (g == 0) {
#pragma unroll
        for (int n = 0; n < NF; ++n) st_cs[wr * 192 + wc * NF * 16 + n * 16 + fr] = cs[n][0];
      }
    }
    WAIT_LGKM0();
    BARRIER();
    STAMP(0, 102);
    if (TYPE == 0) {
      float csv[NF];
#pragma unroll
      for (int n = 0; n < NF; ++n) {
        int c0 = wc * NF * 16 + n * 16 + fr;
        csv[n] = (st_cs[c0] + st_cs[192 + c0]) + (st_cs[384 + c0] + st_cs[576 + c0]);
      }
      bf16x8 xf[NF];
#pragma unroll
      for (int m = 0; m < 2; ++m) {
        int r0 = wr * 32 + (m ^ wc) * 16 + g * 4;
        f32x4 mean = *(LAS f32x4*)(st_rs + r0) * (1.f / D_);
        f32x4 ex2 = *(LAS f32x4*)(st_sq + r0) * (1.f / D_);
        f32x4 rstd, nm;
#pragma unroll
        for (int r = 0; r < 4; ++r) {
          rstd[r] = __builtin_amdgcn_rsqf(fmaxf(ex2[r] - mean[r] * mean[r], 0.f) + 1e-5f);
          nm[r] = -mean[r] * rstd[r];
        }
#pragma unroll
        for (int n = 0; n < NF; ++n) {
          f32x4 y = acc[m][n] * rstd + nm * csv[n];
          bool isk = (n * 16 + fr) < 40;
          float v[4];
#pragma unroll
          for (int r = 0; r < 4; ++r) v[r] = n < 2 ? phi_f(y[r]) : (n == 2 ? (isk ? phi_f(y[r]) : y[r]) : y[r]);
          u32x4 t4 = __builtin_bit_cast(u32x4, xf[n]);
          t4[m * 2] = pack_bf16(v[0], v[1]);
          t4[m * 2 + 1] = pack_bf16(v[2], v[3]);
          xf[n] = __builtin_bit_cast(bf16x8, t4);
        }
      }
      SBAR();
      STAMP(0, 104);
      constexpr int PI[7] = {0, 0, 1, 1, 1, 2, 2};
      constexpr int PJ[7] = {2, 3, 2, 3, 4, 3, 4};
      f32x4 dd[10];
#pragma unroll
      for (int q = 0; q < 7; ++q) dd[q] = mfma16(xf[PI[q]], xf[PJ[q]], f32x4{0, 0, 0, 0});
#pragma unroll
      for (int i = 0; i < 3; ++i) dd[7 + i] = mfma16(xf[i], ones, f32x4{0, 0, 0, 0});
      SBAR();
#pragma unroll
      for (int q = 0; q < 10; ++q) *(LAS f32x4*)(uintptr_t)(otile + wid * 10240 + q * 1024 + lane * 16) = dd[q];
    } else {
      f32x4 csv[NF];
#pragma unroll
      for (int n = 0; n < NF; ++n) {
        int c0 = wc * NF * 16 + n * 16 + g * 4;
        csv[n] = (*(LAS f32x4*)(st_cs + c0) + *(LAS f32x4*)(st_cs + 192 + c0)) + (*(LAS f32x4*)(st_cs + 384 + c0) + *(LAS f32x4*)(st_cs + 576 + c0));
      }
#pragma unroll
      for (int m = 0; m < 2; ++m) {
        int rl = wr * 32 + (m ^ wc) * 16 + fr;
        float mean = st_rs[rl] * (1.f / D_);
        float ex2 = st_sq[rl] * (1.f / D_);
        float rstd = __builtin_amdgcn_rsqf(fmaxf(ex2 - mean * mean, 0.f) + 1e-5f);
        float nm = -mean * rstd;
        if (TYPE == 2 && ct == 0 && m == 0 && g == 0) *(f32x2*)((float*)(p.ws + WS_XST) + (size_t)(rg * 128 + rl) * 2) = f32x2{nm, rstd};
        unsigned dst = otile + rl * OST + (wc * NF * 16 + g * 4) * 2;
#pragma unroll
        for (int n = 0; n < NF; ++n) {
          f32x4 y = acc[m][n] * rstd + csv[n] * nm;
          *(LAS u32x2*)(uintptr_t)(dst + n * 32) = u32x2{pack_bf16(y[0], y[1]), pack_bf16(y[2], y[3])};
        }
      }
    }
    STAMP(0, 105);
  }
  WAIT_LGKM0();
  BARRIER();
  STAMP(0, 106);
  if (TYPE == 0) {
    float* kvp = (float*)(p.ws + WS_KVP) + ((size_t)rg * 32 + ct * 4) * 420;
    for (int i = tid; i < 1680; i += 512) {
      int hl = i / 420, w = i - hl * 420;
      int hi = hl & 1, ci, cj, q;
      if (w < 400) {
        int d = w / 20, e = w - d * 20;
        ci = hi * 20 + d; cj = 40 + hi * 20 + e;
        int pi = ci >> 4, pj = cj >> 4;
        q = pi == 0 ? pj - 2 : (pi == 1 ? pj : pj + 2);
      } else {
        ci = hi * 20 + (w - 400); cj = 0;
        q = 7 + (ci >> 4);
      }
      unsigned a = otile + (hl >> 1) * 10240 + q * 1024 + ((((ci & 15) >> 2) * 16 + (cj & 15)) * 4 + (ci & 3)) * 4;
      float v = (*(LAS float*)(uintptr_t)(a) + *(LAS float*)(uintptr_t)(a + 2 * 10240)) +
                (*(LAS float*)(uintptr_t)(a + 4 * 10240) + *(LAS float*)(uintptr_t)(a + 6 * 10240));
      kvp[i] = v;
    }
  } else {
    u16* dstm = TYPE == 1 ? (u16*)(p.ws + WS_Q) + (size_t)rg * 128 * D_ + ct * 160
                          : (u16*)(p.ws + WS_Y2) + (size_t)rg * 128 * F_ + ct * 192;
    constexpr int ldo = TYPE == 1 ? D_ : F_;
    for (int idx = tid; idx < 128 * CPR; idx += 512) {
      int row = idx / CPR, c = idx - row * CPR;
      u32x4 v = *(LAS u32x4*)(uintptr_t)(otile + row * OST + c * 16);
      *(u32x4*)(dstm + (size_t)row * ldo + c * 8) = v;
    }
  }
}

__global__ __launch_bounds__(S1_THREADS, 3) void stage1_kernel(Params p) {
  extern __shared__ __attribute__((aligned(16))) char shm[];
  unsigned lds0 = (unsigned)(uintptr_t)(LAS char*)shm;
  int b = BIDX, xcd = b & 7, j = b >> 3;
  int rg = (xcd & 1) * 8 + (j & 7);
  int cq = xcd >> 1, ci = j >> 3;
  STAMP(0, 200);
  if (b == 0) {
    for (int i = TIDX; i < 64 * 32; i += S1_THREADS) ((unsigned*)(p.ws + WS_FLAGS))[i] = 0u;
  }
  if (ci < 2) stage1_body<0>(p, rg, cq * 2 + ci, lds0, xcd, j);
  else if (ci == 2) stage1_body<1>(p, rg, cq, lds0, xcd, j);
  else stage1_body<2>(p, rg, cq, lds0, xcd, j);
  STAMP(0, 201);
}

constexpr int K2_SLOT = 8192 + 12288;
constexpr int K2_NS = 7;
constexpr int K2_SCR = K2_NS * K2_SLOT;
constexpr int K2_LDS = K2_SCR + 20480;
constexpr int K2_MID = (K2_NS - 1) * K2_SLOT;
constexpr int K2_THREADS = 1024;
constexpr int T_G = 10, T_A = 42, T_B = 52, T_END = 64;
constexpr unsigned SPIN_CAP = 1u << 22;

typedef __attribute__((address_space(1))) unsigned gu32;
DI void* uni_ptr(const void* q) {
  unsigned long long a = (unsigned long long)q;
  unsigned lo = __builtin_amdgcn_readfirstlane((unsigned)a), hi = __builtin_amdgcn_readfirstlane((unsigned)(a >> 32));
  return (void*)(((unsigned long long)hi << 32) | lo);
}
DI void store16_sc1(void* sbase, unsigned voff, u32x4 v) {
  sbase = uni_ptr(sbase);
  asm volatile("s_nop 4\n\tglobal_store_dwordx4 %0, %1, %2 sc1\n\ts_nop 1" ::"v"(voff), "v"(v), "s"(sbase) : "memory");
}
DI void store8_sc1(void* sbase, unsigned voff, u32x2 v) {
  asm volatile("global_store_dwordx2 %0, %1, %2 sc1\n\ts_nop 1" ::"v"(voff), "v"(v), "s"(sbase) : "memory");
}
DI void glds16_sc1(const void* sbase, unsigned voff, unsigned lds_addr) {
  unsigned sv;
  asm volatile("s_mov_b32 %0, m0\n\ts_mov_b32 m0, %3\n\ts_nop 0\n\tglobal_load_lds_dwordx4 %1, %2 sc1\n\ts_mov_b32 m0, %0"
               : "=&s"(sv) : "v"(voff), "s"(sbase), "s"(lds_addr));
}
DI void wait_flags(unsigned* flags, int nw, unsigned target, int lane) {
  gu32* fp = (gu32*)(flags + (lane < nw ? lane : 0) * 32);
  for (unsigned spins = 0; spins < SPIN_CAP; ++spins) {
    unsigned v = __hip_atomic_load(fp, __ATOMIC_RELAXED, __HIP_MEMORY_SCOPE_AGENT);
    if (__all(v >= target)) break;
    __builtin_amdgcn_s_sleep(1);
  }
  __builtin_amdgcn_fence(__ATOMIC_ACQUIRE, "wavefront");
}

__global__ __launch_bounds__(K2_THREADS, 4) void stage2_kernel(Params p) {
  extern __shared__ __attribute__((aligned(16))) char shm[];
  const unsigned lds0 = (unsigned)(uintptr_t)(LAS char*)shm;
  const int b = BIDX, xcd = b & 7, j = b >> 3;
  const int rg = (xcd & 3) * 8 + (j & 7);
  const int cb = (xcd >> 2) * 4 + (j >> 3);
  const int rg3 = xcd * 4 + (j & 3), cb3 = j >> 2;
  const int tid = TIDX, lane = tid & 63;
  const int wid = __builtin_amdgcn_readfirstlane(tid >> 6);
  const int fr = lane & 15, g = lane >> 4;
  unsigned* flags = (unsigned*)(p.ws + WS_FLAGS);
  constexpr int CPR = 12;
  STAMP(1, 200);

  if (wid >= 12) {
    const int st = tid - 768, sw = wid - 12;
    const int mcb = b & 7;
    const unsigned scr = lds0 + K2_SCR;
    LAS float* zt = (LAS float*)(uintptr_t)(scr + 18432);
#define SB(n) do { _Pragma("unroll 1") for (int i_ = 0; i_ < (n); ++i_) BARRIER(); } while (0)
#define FLAG_ADD_S(off) do { if (st == 0) __hip_atomic_fetch_add((gu32*)(flags + (off)), 1u, __ATOMIC_RELAXED, __HIP_MEMORY_SCOPE_AGENT); } while (0)
    u32x4 w0, w1;
    u32x2 qw[5];
    {
      const u16* wsrc = b < 128 ? p.wo + (size_t)(b >> 3) * 40 * D_ : p.w_out + (size_t)((b - 128) >> 3) * 48 * D_;
      const int nch = b < 128 ? 400 : 480;
      int i1 = st + 256 < nch ? st + 256 : 0;
      w0 = *(const u32x4*)(wsrc + (st / 10) * D_ + mcb * 80 + (st % 10) * 8);
      w1 = *(const u32x4*)(wsrc + (i1 / 10) * D_ + mcb * 80 + (i1 % 10) * 8);
      const u16* qsrc = (const u16*)(p.ws + WS_Q) + (size_t)((b >> 1) * 16 + (st >> 4)) * D_ + (b & 1) * 320 + (st & 15) * 20;
#pragma unroll
      for (int q = 0; q < 5; ++q) qw[q] = *(const u32x2*)(qsrc + q * 4);
    }
    SB(3);
    float pq[20];
#pragma unroll
    for (int q = 0; q < 3; ++q) {
      pq[q * 4] = phi_f(bf_lo(qw[q][0])); pq[q * 4 + 1] = phi_f(bf_hi(qw[q][0]));
      pq[q * 4 + 2] = phi_f(bf_lo(qw[q][1])); pq[q * 4 + 3] = phi_f(bf_hi(qw[q][1]));
    }
    SBAR();
    SB(1);
#pragma unroll
    for (int q = 3; q < 5; ++q) {
      pq[q * 4] = phi_f(bf_lo(qw[q][0])); pq[q * 4 + 1] = phi_f(bf_hi(qw[q][0]));
      pq[q * 4 + 2] = phi_f(bf_lo(qw[q][1])); pq[q * 4 + 3] = phi_f(bf_hi(qw[q][1]));
    }
    *(LAS u32x4*)(uintptr_t)(scr + 8192 + st * 16) = w0;
    if (st + 256 < (b < 128 ? 400 : 480)) *(LAS u32x4*)(uintptr_t)(scr + 8192 + (st + 256) * 16) = w1;
    if (b < 128) {
      *(LAS u32x4*)(uintptr_t)(scr + st * 16) = u32x4{0, 0, 0, 0};
      *(LAS u32x4*)(uintptr_t)(scr + 4096 + st * 16) = u32x4{0, 0, 0, 0};
      if (st < 240) *(LAS u32x4*)(uintptr_t)(scr + 8192 + 6400 + st * 16) = u32x4{0, 0, 0, 0};
    }
    WAIT_LGKM0();
    SB(3);
    if (sw == 0) wait_flags(flags, 16, 16u, lane);
    SB(1);
    u32x4 tb0, tb1;
    {
      unsigned o0 = (((b < 128 ? b >> 3 : 0) * 2 + (st < 200 ? st / 100 : 0)) * 420 + (st % 100) * 4) * 4;
      int i4 = (st < 80 ? st : 0) * 4;
      unsigned o1 = (((b & 1) * 16 + i4 / 20) * 420 + 400 + i4 % 20) * 4;
      asm volatile("global_load_dwordx4 %0, %1, %2 sc1" : "=v"(tb0) : "v"(o0), "s"(p.ws + WS_KVR));
      asm volatile("global_load_dwordx4 %0, %1, %2 sc1" : "=v"(tb1) : "v"(o1), "s"(p.ws + WS_KVR));
    }
    const int q4 = fr >> 2, pp = fr & 3;
    if (b >= 128) {
#pragma unroll
      for (int it = 0; it < 2; ++it) {
        int blk = (it * 4 + sw) * 4 + g;
        int bb = blk < 30 ? blk : 0;
        int kc = bb / 5, nf = bb - kc * 5;
        unsigned wa = scr + 8192 + (kc * 8 + q4) * 160 + nf * 32 + pp * 8;
        bf16x8 v = cat8(lds_tr1(wa), lds_tr1(wa + 4 * 160));
        if (blk < 30) store16_sc1(p.ws + WS_B3, ((mcb * 176 + 80 + ((b - 128) >> 3) * 6 + kc) * 80 + nf * 16 + fr) * 16, __builtin_bit_cast(u32x4, v));
      }
    }
    SB(5);
    asm volatile("s_waitcnt vmcnt(0)" : "+v"(tb0), "+v"(tb1) :: "memory");
    if (st < 80) *(LAS u32x4*)(zt + st * 4) = tb1;
    if (b < 128 && st < 200) {
      int hh = st / 100, w = (st - hh * 100) * 4, dl = w / 20, el = w - dl * 20;
      int d = hh * 20 + dl, e = hh * 20 + el;
      int row = (d >> 5) * 32 + ((d >> 2) & 1) * 16 + ((d >> 3) & 3) * 4 + (d & 3);
      f32x4 kf = __builtin_bit_cast(f32x4, tb0);
      unsigned a = scr + row * 128 + ((((e >> 3) ^ ((row >> 1) & 7)) << 4) | ((e & 4) << 1));
      *(LAS u32x2*)(uintptr_t)a = u32x2{pack_bf16(kf[0], kf[1]), pack_bf16(kf[2], kf[3])};
    }
    WAIT_LGKM0();
    SB(1);
    {
      float den = 0.f;
#pragma unroll
      for (int q = 0; q < 5; ++q) {
        f32x4 zz = *(LAS f32x4*)(zt + (st & 15) * 20 + q * 4);
        den += pq[q * 4] * zz[0] + pq[q * 4 + 1] * zz[1] + pq[q * 4 + 2] * zz[2] + pq[q * 4 + 3] * zz[3];
      }
      float inv = 1.f / (den + 1e-6f);
#pragma unroll
      for (int q = 0; q < 5; ++q)
        qw[q] = u32x2{pack_bf16(pq[q * 4] * inv, pq[q * 4 + 1] * inv), pack_bf16(pq[q * 4 + 2] * inv, pq[q * 4 + 3] * inv)};
    }
    SBAR();
    SB(1);
#pragma unroll
    for (int it = 0; it < 3; ++it) {
      if (b < 128) {
        int unit = sw + it * 4;
        int u = unit < 10 ? unit : 0;
        int db = u / 5, nf = u - db * 5;
        f32x4 d0 = {0, 0, 0, 0}, d1 = {0, 0, 0, 0};
#pragma unroll
        for (int ks = 0; ks < 2; ++ks) {
          unsigned wa = scr + 8192 + (ks * 32 + g * 8 + q4) * 160 + nf * 32 + pp * 8;
          bf16x8 bw = cat8(lds_tr1(wa), lds_tr1(wa + 4 * 160));
          unsigned aa = scr + (db * 32 + fr) * 128 + (((ks * 4 + g) ^ ((fr >> 1) & 7)) << 4);
          d0 = mfma16(lds_b128(aa), bw, d0);
          d1 = mfma16(lds_b128(aa + 2048), bw, d1);
        }
        int kc = db * 4 + g;
        if (unit < 10 && kc < 5) {
          u32x4 o = {pack_bf16(d0[0], d0[1]), pack_bf16(d0[2], d0[3]), pack_bf16(d1[0], d1[1]), pack_bf16(d1[2], d1[3])};
          store16_sc1(p.ws + WS_B3, ((mcb * 176 + (b >> 3) * 5 + kc) * 80 + nf * 16 + fr) * 16, o);
        }
      }
      WAIT_LGKM0();
      SB(1);
    }
    constexpr int PST = 640 + 16;
    {
      unsigned a = scr + (st >> 4) * PST + (st & 15) * 40;
#pragma unroll
      for (int q = 0; q < 5; ++q) *(LAS u32x2*)(uintptr_t)(a + q * 8) = qw[q];
    }
    WAIT_LGKM0();
    SB(1);
    {
      const int prg = b >> 3, r0 = ((b >> 1) & 3) * 16;
#pragma unroll
      for (int it = 0; it < 3; ++it) {
        int idx = st + it * 256;
        if (idx < 640) {
          int ktl = idx >> 7, row = (idx >> 3) & 15, cp = idx & 7;
          int c = cp ^ (((r0 + row) >> 1) & 7);
          u32x4 v = *(LAS u32x4*)(uintptr_t)(scr + row * PST + (ktl * 8 + c) * 16);
          store16_sc1(p.ws + WS_UA, ((prg * 22 + (b & 1) * 5 + ktl) * 64 + r0 + row) * 128 + cp * 16, v);
        }
      }
    }
    SB(5);
    WAIT_VM(0);
    SB(1);
    FLAG_ADD_S(512 + (b & 15) * 32);
    SB(12);
    if (sw == 0) wait_flags(flags + 512, 16, 16u, lane);
    SB(7);
    {
      constexpr int OST = 96 * 2 + 16;
#pragma unroll
      for (int it = 0; it < 3; ++it) {
        int idx = st + it * 256;
        int row = idx / CPR, c = idx - row * CPR;
        u32x4 v = *(LAS u32x4*)(uintptr_t)(scr + row * OST + c * 16);
        int cg = cb * 12 + c;
        store16_sc1(p.ws + WS_UA, ((rg * 22 + 10 + (cg >> 3)) * 64 + row) * 128 + (((cg & 7) ^ ((row >> 1) & 7)) << 4), v);
      }
    }
    SB(2);
    WAIT_VM(0);
    SB(1);
    FLAG_ADD_S(1024 + rg * 32);
    SB(6);
    if (sw == 0) wait_flags(flags + 1024 + rg3 * 32, 1, 8u, lane);
    SB(1);
  } else if (wid >= 8) {
    const int lw = wid - 8, lt = tid - 512;
    unsigned aoff1, aoff2, boff1[3];
    {
      int row = lw * 8 + (lane >> 3), c = (lane & 7) ^ ((row >> 1) & 7);
      aoff1 = ((rg * 64 + row) * D_ + c * 8) * 2;
      aoff2 = ((rg * 64 + row) * S_ + c * 8) * 2;
    }
#pragma unroll
    for (int i = 0; i < 3; ++i) {
      int slot = i * 256 + lt;
      int k = slot / CPR;
      boff1[i] = (k * (2 * F_) + cb * 96 + bswz<false>(k, slot - k * CPR) * 8) * 2;
    }
    const unsigned boff2 = lt * 16;
    const u16* zpanel = (const u16*)(p.ws + WS_Z2N) + (size_t)cb * S_ * 96;
    const u16* bpanel = (const u16*)(p.ws + WS_B3) + (size_t)cb3 * KA_ * 80;
    const u16* ua = (const u16*)(p.ws + WS_UA) + (size_t)rg3 * 22 * 4096;
    const bool b3 = lw < 2;
    const unsigned lbase = lds0 + lw * 1024;
    auto ld_z1_a = [&](int t, int sl) {
#pragma unroll
      for (int i = 0; i < 2; ++i) glds16(p.x + t * 64 + i * 32 * D_, aoff1, lbase + sl * K2_SLOT + i * 4096);
    };
    auto ld_z1_b = [&](int t, int sl) {
#pragma unroll
      for (int i = 0; i < 3; ++i) glds16(p.w_in + (size_t)t * 64 * (2 * F_), boff1[i], lbase + sl * K2_SLOT + 8192 + i * 4096);
    };
    auto ld_g_a = [&](int t, int sl) {
#pragma unroll
      for (int i = 0; i < 2; ++i) glds16(p.w_sp + t * 64 + i * 32 * S_, aoff2, lbase + sl * K2_SLOT + i * 4096);
    };
    auto ld_g_b = [&](int t, int sl) {
#pragma unroll
      for (int i = 0; i < 3; ++i) glds16_sc1(zpanel + (size_t)t * 64 * 96 + i * 2048, boff2, lbase + sl * K2_SLOT + 8192 + i * 4096);
    };
    auto ld_o_a = [&](int t, int sl) {
#pragma unroll
      for (int i = 0; i < 2; ++i) glds16_sc1(ua + t * 4096 + i * 2048, boff2, lbase + sl * K2_SLOT + i * 4096);
    };
    auto ld_o_b = [&](int t, int sl) {
#pragma unroll
      for (int i = 0; i < 2; ++i) glds16_sc1(bpanel + (size_t)t * 64 * 80 + i * 2048, boff2, lbase + sl * K2_SLOT + 8192 + i * 4096);
      if (b3) glds16_sc1(bpanel + (size_t)t * 64 * 80 + 4096, boff2, lbase + sl * K2_SLOT + 8192 + 8192);
    };
    int sl = 0;
    auto nx = [&]() { sl = sl == K2_NS - 1 ? 0 : sl + 1; };
#pragma unroll 1
    for (int t = 0; t < 2; ++t) { ld_z1_a(t, sl); ld_z1_b(t, sl); nx(); }
    WAIT_VM(5); BARRIER();
#pragma unroll 1
    for (int t = 2; t < 4; ++t) { ld_z1_a(t, sl); ld_z1_b(t, sl); nx(); }
    WAIT_VM(10); BARRIER();
#pragma unroll 1
    for (int t = 4; t < 6; ++t) { ld_z1_a(t, sl); ld_z1_b(t, sl); nx(); }
#pragma unroll 1
    for (int t = 6; t < 10; ++t) { ld_z1_a(t, sl); ld_z1_b(t, sl); nx(); WAIT_VM(20); BARRIER(); }
    ld_g_a(0, 3); WAIT_VM(17); BARRIER();
    ld_g_a(1, 4); WAIT_VM(14); BARRIER();
    ld_g_b(0, 3); ld_g_b(1, 4);
    sl = 5;
#pragma unroll 1
    for (int t = 2; t < 32; ++t) {
      ld_g_a(t, sl); ld_g_b(t, sl); nx();
      if (t == 4) WAIT_VM(18); else WAIT_VM(20);
      BARRIER();
    }
#pragma unroll 1
    for (int t = 0; t < 10; ++t) {
      ld_o_a(t, sl); ld_o_b(t, sl); nx();
      if (b3) WAIT_VM(20); else WAIT_VM(16);
      BARRIER();
    }
    const int sl52 = sl;
    ld_o_b(10, sl); nx(); if (b3) WAIT_VM(18); else WAIT_VM(14); BARRIER();
    ld_o_b(11, sl); nx(); if (b3) WAIT_VM(16); else WAIT_VM(12); BARRIER();
    ld_o_b(12, sl); nx(); if (b3) WAIT_VM(14); else WAIT_VM(10); BARRIER();
    ld_o_b(13, sl); nx(); if (b3) WAIT_VM(12); else WAIT_VM(8); BARRIER();
    ld_o_b(14, sl); nx(); BARRIER();
    ld_o_b(15, sl); nx(); BARRIER();
    {
      int s2 = sl52;
#pragma unroll 1
      for (int t = 10; t < 16; ++t) { ld_o_a(t, s2); s2 = s2 == K2_NS - 1 ? 0 : s2 + 1; }
    }
    WAIT_VM(10); BARRIER();
    WAIT_VM(8); BARRIER();
#pragma unroll 1
    for (int t = 16; t < 22; ++t) {
      ld_o_a(t, sl); ld_o_b(t, sl); nx();
      if (t == 16) WAIT_VM(10); else if (t == 17) WAIT_VM(12); else if (t == 18) WAIT_VM(14); else WAIT_VM(16);
      BARRIER();
    }
    if (b3) WAIT_VM(15); else WAIT_VM(12); BARRIER();
    if (b3) WAIT_VM(10); else WAIT_VM(8); BARRIER();
    if (b3) WAIT_VM(5); else WAIT_VM(4); BARRIER();
    WAIT_VM(0); BARRIER();
    BARRIER();
    BARRIER();
  } else {
    const int wr = wid >> 1, wc = wid & 1;
    const int kh = wid >> 2, wm = wid & 3;
    const int mcb = b & 7;
    const unsigned scr = lds0 + K2_SCR;
    LAS float* sx = (LAS float*)(uintptr_t)scr;
    LAS float* cst = (LAS float*)(uintptr_t)(scr + 16384);
    LAS float* krp = (LAS float*)(uintptr_t)(scr + 16384);
    const int urow = wr * 16 + fr;

    constexpr int ZST = 1536 + 32;
    u32x2 yv[3];
    u32x4 tb0;
    {
      unsigned oy = ((b * 8 + wid) * F_ + lane * 4) * 2;
#pragma unroll
      for (int i = 0; i < 3; ++i) asm volatile("global_load_dwordx2 %0, %1, %2" : "=v"(yv[i]) : "v"(oy + i * 512), "s"(p.ws + WS_Y2));
      unsigned ok = (((tid >> 4) & 15) * 13440 + (b < 210 ? b : 0) * 64 + (tid & 15) * 4) * 4;
      asm volatile("global_load_dwordx4 %0, %1, %2" : "=v"(tb0) : "v"(ok), "s"(p.ws + WS_KVP));
    }
    auto parta_compute = [&]() {
      asm volatile("s_waitcnt vmcnt(0)" : "+v"(yv[0]), "+v"(yv[1]), "+v"(yv[2]), "+v"(tb0) :: "memory");
      float z[12];
#pragma unroll
      for (int i = 0; i < 3; ++i)
#pragma unroll
        for (int q = 0; q < 2; ++q) {
          unsigned w = pack_bf16(gelu_f(bf_lo(yv[i][q])), gelu_f(bf_hi(yv[i][q])));
          z[i * 4 + q * 2] = bf_lo(w);
          z[i * 4 + q * 2 + 1] = bf_hi(w);
        }
      float s = 0.f;
#pragma unroll
      for (int i = 0; i < 12; ++i) s += z[i];
      s = wave_sum(s);
      float mean = s * (1.f / F_), v2 = 0.f;
#pragma unroll
      for (int i = 0; i < 12; ++i) { z[i] -= mean; v2 = __builtin_fmaf(z[i], z[i], v2); }
      v2 = wave_sum(v2);
      float rstd = __builtin_amdgcn_rsqf(v2 * (1.f / F_) + 1e-5f);
#pragma unroll
      for (int i = 0; i < 3; ++i)
        *(LAS u32x2*)(uintptr_t)(scr + wid * ZST + (i * 256 + lane * 4) * 2) =
            u32x2{pack_bf16(z[i * 4] * rstd, z[i * 4 + 1] * rstd), pack_bf16(z[i * 4 + 2] * rstd, z[i * 4 + 3] * rstd)};
      if (wid < 4) *(LAS u32x4*)(krp + tid * 4) = tb0;
      WAIT_LGKM0();
    };
    auto parta_store = [&]() {
#pragma unroll
      for (int it = 0; it < 2; ++it) {
        int grp64 = it ? 8 + (wid & 3) : wid;
        int q = fr >> 2, pp = fr & 3;
        unsigned a = scr + q * ZST + (grp64 * 64 + g * 16 + pp * 4) * 2;
        bf16x8 v = cat8(lds_tr1(a), lds_tr1(a + 4 * ZST));
        int f = grp64 * 64 + lane, c = f / 96, n = f - c * 96;
        if (it == 0 || wid < 4) store16_sc1(p.ws + WS_Z2N, ((c * 256 + b) * 96 + n) * 16, __builtin_bit_cast(u32x4, v));
      }
      if (tid >= 448 && tid < 464 && b < 210) {
        int v = tid - 448;
        f32x4 t4 = {0, 0, 0, 0};
#pragma unroll
        for (int q = 0; q < 16; ++q) t4 += *(LAS f32x4*)(krp + q * 64 + v * 4);
        store16_sc1(p.ws + WS_KVR, (b * 64 + v * 4) * 4, __builtin_bit_cast(u32x4, t4));
      }
    };

    unsigned abase[2], baddr1[3], baddr2, abase3, baddr3;
    {
      int x7 = (fr >> 1) & 7;
#pragma unroll
      for (int ks = 0; ks < 2; ++ks) abase[ks] = lds0 + (wr * 16 + fr) * 128 + (((ks * 4 + g) ^ x7) << 4);
      int q = fr >> 2, pp = fr & 3;
#pragma unroll
      for (int n = 0; n < 3; ++n) {
        int k = 8 * g + q;
        baddr1[n] = lds0 + 8192 + (k * CPR + bswz<false>(k, 2 * (wc * 3 + n) + (pp >> 1))) * 16 + (pp & 1) * 8;
      }
      baddr2 = lds0 + 8192 + (g * 96 + wc * 48 + fr) * 16;
      abase3 = lds0 + (wm * 16 + fr) * 128 + (((kh * 4 + g) ^ x7) << 4);
      baddr3 = lds0 + 8192 + ((kh * 4 + g) * 80 + fr) * 16;
    }
    f32x4 acc1[3], acc2[3], cs[3], acc3[5];
#pragma unroll
    for (int n = 0; n < 3; ++n) { acc1[n] = f32x4{0, 0, 0, 0}; acc2[n] = f32x4{0, 0, 0, 0}; cs[n] = f32x4{0, 0, 0, 0}; }
#pragma unroll
    for (int n = 0; n < 5; ++n) acc3[n] = f32x4{0, 0, 0, 0};
    const short one = 0x3f80;
    bf16x8 ones = {one, one, one, one, one, one, one, one};
    asm volatile("" : "+v"(ones));

    bf16x8 fA[8], fB[8];
    unsigned slot = 0;
    int z1t = 0;
    auto adv = [&]() { slot = slot == (K2_NS - 1) * K2_SLOT ? 0 : slot + K2_SLOT; };
    auto ldf = [&](bf16x8 (&f)[8], int mode, int i, unsigned& tmp) {
      if (mode == 2) {
        if (i == 0) f[0] = lds_b128(pinv(abase3 + slot));
        else if (i < 6) { if (i == 1) tmp = pinv(baddr3 + slot); f[1 + i] = lds_b128(tmp + (i - 1) * 256); }
      } else {
        if (i == 0 || i == 4) f[i >> 2] = lds_b128(pinv(abase[i >> 2] + slot));
        else {
          int ks = i >> 2, n = (i & 3) - 1;
          if (mode == 0) {
            unsigned a = pinv(baddr1[n] + slot) + ks * 32 * CPR * 16;
            f[2 + ks * 3 + n] = cat8(lds_tr1(a), lds_tr1(a + 4 * CPR * 16));
          } else {
            if (i == 1) tmp = pinv(baddr2 + slot);
            f[2 + ks * 3 + n] = lds_b128(tmp + ks * 4 * 96 * 16 + n * 256);
          }
        }
      }
    };
    auto iter = [&](bf16x8 (&fc)[8], bf16x8 (&fn)[8], auto mc, auto mn) {
      constexpr int MC = decltype(mc)::value, MN = decltype(mn)::value;
      unsigned tmp = 0;
#pragma unroll
      for (int i = 0; i < 8; ++i) {
        if (MC == 2) {
          if (i < 5) acc3[i] = mfma16(fc[2 + i], fc[0], acc3[i]);
        } else if (i < 6) {
          int ks = i / 3, n = i % 3;
          if (MC == 0) acc1[n] = mfma16(fc[2 + i], fc[ks], acc1[n]);
          else acc2[n] = mfma16(fc[2 + i], fc[ks], acc2[n]);
        }
        if (MN < 3) ldf(fn, MN, i, tmp);
        SBAR();
      }
      if (MC == 0) {
        if (wr == (z1t & 3)) {
#pragma unroll
          for (int i = 0; i < 6; ++i) cs[i % 3] = mfma16(fc[2 + i], ones, cs[i % 3]);
        }
        ++z1t;
        SBAR();
      }
      if (MN < 3) adv();
    };
    using I0 = std::integral_constant<int, 0>;
    using I1 = std::integral_constant<int, 1>;
    using I2 = std::integral_constant<int, 2>;
    using I3 = std::integral_constant<int, 3>;
#define FLAG_ADD(off) do { if (tid == 0) __hip_atomic_fetch_add((gu32*)(flags + (off)), 1u, __ATOMIC_RELAXED, __HIP_MEMORY_SCOPE_AGENT); } while (0)
    const f32x2 xs = *(const f32x2*)((const float*)(p.ws + WS_XST) + (size_t)(rg * 64 + urow) * 2);
    const float bias = bf_lo(p.b_sp[rg * 64 + urow]);
    parta_compute();
    STAMP(1, 209);
    BARRIER();
    STAMP(1, 0);
    parta_store();
    {
      unsigned tmp = 0;
#pragma unroll
      for (int i = 0; i < 8; ++i) ldf(fA, 0, i, tmp);
      adv();
    }
    BARRIER();
    iter(fA, fB, I0{}, I0{}); WAIT_VM(0); BARRIER();
    FLAG_ADD((b & 15) * 32);
    STAMP(1, 1);
    iter(fB, fA, I0{}, I0{}); BARRIER();
    iter(fA, fB, I0{}, I0{}); BARRIER();
    iter(fB, fA, I0{}, I0{}); BARRIER();
    STAMP(1, 4);
    iter(fA, fB, I0{}, I0{}); BARRIER();
    iter(fB, fA, I0{}, I0{}); BARRIER();
    STAMP(1, 6);
    iter(fA, fB, I0{}, I0{}); BARRIER();
    iter(fB, fA, I0{}, I0{}); BARRIER();
    iter(fA, fB, I0{}, I0{}); BARRIER();
    iter(fB, fA, I0{}, I1{}); BARRIER();
    STAMP(1, 10);
#pragma unroll 1
    for (int t = 10; t < 18; t += 2) {
      iter(fA, fB, I1{}, I1{}); BARRIER();
      iter(fB, fA, I1{}, I1{}); BARRIER();
    }
    STAMP(1, 18);
    iter(fA, fB, I1{}, I1{});
    if (fr == 0) {
#pragma unroll
      for (int n = 0; n < 3; ++n) *(LAS f32x4*)(cst + wr * 96 + wc * 48 + n * 16 + g * 4) = cs[n];
    }
    WAIT_LGKM0();
    BARRIER();
    auto z1act = [&](int n) {
      SBAR();
      int c0 = wc * 48 + n * 16 + g * 4;
      f32x4 c4 = (*(LAS f32x4*)(cst + c0) + *(LAS f32x4*)(cst + 96 + c0)) + (*(LAS f32x4*)(cst + 192 + c0) + *(LAS f32x4*)(cst + 288 + c0));
#pragma unroll
      for (int r = 0; r < 4; ++r) acc1[n][r] = gelu_f(__builtin_fmaf(acc1[n][r], xs[1], c4[r] * xs[0]));
      asm volatile("" : "+v"(acc1[n]));
      SBAR();
    };
    iter(fB, fA, I1{}, I1{}); z1act(0); BARRIER();
    iter(fA, fB, I1{}, I1{}); z1act(1); BARRIER();
    iter(fB, fA, I1{}, I1{}); z1act(2); BARRIER();
    iter(fA, fB, I1{}, I1{}); BARRIER();
    u32x4 xv0, xv1;
    {
      int i0 = tid, i1 = tid < 128 ? tid + 512 : tid;
      xv0 = *(const u32x4*)(p.x + (size_t)(rg3 * 64 + i0 / 10) * D_ + cb3 * 80 + (i0 % 10) * 8);
      xv1 = *(const u32x4*)(p.x + (size_t)(rg3 * 64 + i1 / 10) * D_ + cb3 * 80 + (i1 % 10) * 8);
    }
    iter(fB, fA, I1{}, I1{}); BARRIER();
#pragma unroll 1
    for (int t = 24; t < 34; t += 2) {
      iter(fA, fB, I1{}, I1{}); BARRIER();
      iter(fB, fA, I1{}, I1{}); BARRIER();
    }
    iter(fA, fB, I1{}, I1{}); BARRIER();
    iter(fB, fA, I1{}, I1{}); BARRIER();
#pragma unroll 1
    for (int t = 36; t < 40; t += 2) {
      iter(fA, fB, I1{}, I1{}); BARRIER();
      iter(fB, fA, I1{}, I1{}); BARRIER();
    }
    iter(fA, fB, I1{}, I1{}); BARRIER();
    iter(fB, fA, I1{}, I2{});
    STAMP(1, 42);
    constexpr int OST = 96 * 2 + 16;
    {
      unsigned dst = scr + urow * OST + (wc * 48 + g * 4) * 2;
#pragma unroll
      for (int n = 0; n < 3; ++n) {
        float v[4];
#pragma unroll
        for (int r = 0; r < 4; ++r) v[r] = acc1[n][r] * (acc2[n][r] + bias);
        *(LAS u32x2*)(uintptr_t)(dst + n * 32) = u32x2{pack_bf16(v[0], v[1]), pack_bf16(v[2], v[3])};
      }
    }
    SBAR();
    WAIT_LGKM0();
    BARRIER();
    iter(fA, fB, I2{}, I2{}); BARRIER();
    STAMP(1, 43);
    iter(fB, fA, I2{}, I2{}); BARRIER();
    iter(fA, fB, I2{}, I2{}); BARRIER();
    iter(fB, fA, I2{}, I2{}); BARRIER();
    STAMP(1, 46);
#pragma unroll 1
    for (int t = 46; t < 50; t += 2) {
      iter(fA, fB, I2{}, I2{}); BARRIER();
      iter(fB, fA, I2{}, I2{}); BARRIER();
    }
    iter(fA, fB, I2{}, I2{}); BARRIER();
    STAMP(1, 51);
    iter(fB, fA, I2{}, I3{});
    BARRIER();
    STAMP(1, 52);
    BARRIER();
    {
      unsigned tmp = 0;
#pragma unroll
      for (int i = 0; i < 8; ++i) ldf(fA, 2, i, tmp);
      adv();
    }
    BARRIER();
    STAMP(1, 53);
#pragma unroll 1
    for (int t = T_B; t < T_END; t += 2) {
      iter(fA, fB, I2{}, I2{}); BARRIER();
      iter(fB, fA, I2{}, I2{});
      if (t == T_END - 2) WAIT_LGKM0();
      BARRIER();
    }
    STAMP(1, 100);
    constexpr int OS3 = 80 * 4 + 16;
    {
      unsigned dst = lds0 + kh * (64 * OS3) + (wm * 16 + fr) * OS3 + g * 16;
#pragma unroll
      for (int n = 0; n < 5; ++n) *(LAS f32x4*)(uintptr_t)(dst + n * 64) = acc3[n];
    }
    WAIT_LGKM0();
    BARRIER();
#pragma unroll
    for (int it = 0; it < 2; ++it) {
      int idx = tid + it * 512;
      if (idx < 640) {
        int row = idx / 10, c = idx - row * 10;
        u32x4 xv = it ? xv1 : xv0;
        unsigned la = lds0 + row * OS3 + c * 32;
        f32x4 a = *(LAS f32x4*)(uintptr_t)(la) + *(LAS f32x4*)(uintptr_t)(la + 64 * OS3);
        f32x4 c4 = *(LAS f32x4*)(uintptr_t)(la + 16) + *(LAS f32x4*)(uintptr_t)(la + 64 * OS3 + 16);
        u32x4 o = {pack_bf16(bf_lo(xv[0]) + a[0], bf_hi(xv[0]) + a[1]), pack_bf16(bf_lo(xv[1]) + a[2], bf_hi(xv[1]) + a[3]),
                   pack_bf16(bf_lo(xv[2]) + c4[0], bf_hi(xv[2]) + c4[1]), pack_bf16(bf_lo(xv[3]) + c4[2], bf_hi(xv[3]) + c4[3])};
        *(u32x4*)(p.out + (size_t)(rg3 * 64 + row) * D_ + cb3 * 80 + c * 8) = o;
      }
    }
    STAMP(1, 201);
  }
}

extern "C" void kernel_launch(void* const* d_in, const int* in_sizes, int n_in, void* d_out, int out_size, void* d_ws,
                              size_t ws_size, hipStream_t stream) {
  Params p;
  p.x = (const u16*)d_in[0];
  p.wqkv = (const u16*)d_in[1];
  p.wo = (const u16*)d_in[2];
  p.w_in = (const u16*)d_in[4];
  p.w_sp = (const u16*)d_in[5];
  p.b_sp = (const u16*)d_in[6];
  p.w_out = (const u16*)d_in[7];
  p.out = (u16*)d_out;
  p.ws = (char*)d_ws;
  (void)hipFuncSetAttribute((const void*)stage1_kernel, hipFuncAttributeMaxDynamicSharedMemorySize, S1_LDS);
  (void)hipFuncSetAttribute((const void*)stage2_kernel, hipFuncAttributeMaxDynamicSharedMemorySize, K2_LDS);
  stage1_kernel<<<256, S1_THREADS, S1_LDS, stream>>>(p);
  stage2_kernel<<<256, K2_THREADS, K2_LDS, stream>>>(p);
}

#if defined(__HIP_DEVICE_COMPILE__) && __HIP_DEVICE_COMPILE__
#pragma clang attribute pop
#endif
```

```cpp
#if defined(__HIP_DEVICE_COMPILE__) && __HIP_DEVICE_COMPILE__
#pragma clang attribute push(__attribute__((target("no-packed-fp32-ops"))), apply_to = function)
#endif
#include <hip/hip_runtime.h>
#include <hip/hip_bf16.h>
#include <stdint.h>
#include <type_traits>

typedef short bf16x8 __attribute__((ext_vector_type(8)));
typedef short s16x4 __attribute__((ext_vector_type(4)));
typedef float f32x4 __attribute__((ext_vector_type(4)));
typedef float f32x2 __attribute__((ext_vector_type(2)));
typedef unsigned u32x4 __attribute__((ext_vector_type(4)));
typedef unsigned u32x2 __attribute__((ext_vector_type(2)));
typedef __bf16 bf16x2_t __attribute__((ext_vector_type(2)));
typedef unsigned short u16;

#define TIDX ((int)__builtin_amdgcn_workitem_id_x())
#define BIDX ((int)__builtin_amdgcn_workgroup_id_x())
#define LAS __attribute__((address_space(3)))
#define DI __device__ __forceinline__

constexpr int S_ = 2048, D_ = 640, H_ = 32, DH_ = 20, F_ = 768, KA_ = D_ + F_;

constexpr size_t WS_Y2 = 0;
constexpr size_t WS_Q = WS_Y2 + (size_t)S_ * F_ * 2;
constexpr size_t WS_UA = WS_Q + (size_t)S_ * D_ * 2;
constexpr size_t WS_B3 = WS_UA + (size_t)S_ * KA_ * 2;
constexpr size_t WS_KVP = WS_B3 + (size_t)KA_ * D_ * 2;
constexpr size_t WS_XST = WS_KVP + (size_t)16 * 32 * 420 * 4;
constexpr size_t WS_Z2N = WS_XST + (size_t)S_ * 2 * 4;
constexpr size_t WS_END = WS_Z2N + (size_t)S_ * F_ * 2;
constexpr size_t WS_FLAGS = WS_END;
constexpr size_t WS_KVR = WS_FLAGS + 64 * 32 * 4;

DI unsigned pack_bf16(float a, float b) {
  f32x2 v = {a, b};
  bf16x2_t r = __builtin_convertvector(v, bf16x2_t);
  return __builtin_bit_cast(unsigned, r);
}
DI float bf_lo(unsigned w) { return __builtin_bit_cast(float, w << 16); }
DI float bf_hi(unsigned w) { return __builtin_bit_cast(float, w & 0xffff0000u); }
DI float phi_f(float t) {
  return (fmaxf(t, 0.f) + 1e-6f) + __builtin_amdgcn_exp2f(fminf(t, 0.f) * 1.4426950408889634f);
}
DI float gelu_f(float x) {
  constexpr float c1 = -2.f * 0.7978845608028654f * 1.4426950408889634f;
  constexpr float c3 = c1 * 0.044715f;
  float a = x * __builtin_fmaf(x * x, c3, c1);
  return x * __builtin_amdgcn_rcpf(1.f + __builtin_amdgcn_exp2f(a));
}
DI float wave_sum(float v) {
  v += __builtin_bit_cast(float, __builtin_amdgcn_update_dpp(0, __builtin_bit_cast(int, v), 0xB1, 0xf, 0xf, false));
  v += __builtin_bit_cast(float, __builtin_amdgcn_update_dpp(0, __builtin_bit_cast(int, v), 0x4E, 0xf, 0xf, false));
  v += __builtin_bit_cast(float, __builtin_amdgcn_update_dpp(0, __builtin_bit_cast(int, v), 0x141, 0xf, 0xf, false));
  v += __builtin_bit_cast(float, __builtin_amdgcn_update_dpp(0, __builtin_bit_cast(int, v), 0x140, 0xf, 0xf, false));
  int iv = __builtin_bit_cast(int, v);
  float r0 = __builtin_bit_cast(float, __builtin_amdgcn_readlane(iv, 0)), r1 = __builtin_bit_cast(float, __builtin_amdgcn_readlane(iv, 16));
  float r2 = __builtin_bit_cast(float, __builtin_amdgcn_readlane(iv, 32)), r3 = __builtin_bit_cast(float, __builtin_amdgcn_readlane(iv, 48));
  return (r0 + r1) + (r2 + r3);
}
DI f32x4 mfma16(bf16x8 a, bf16x8 b, f32x4 c) { return __builtin_amdgcn_mfma_f32_16x16x32_bf16(a, b, c, 0, 0, 0); }
DI bf16x8 lds_b128(unsigned addr) { return *(LAS bf16x8*)(uintptr_t)addr; }
DI s16x4 lds_tr1(unsigned a) { return __builtin_amdgcn_ds_read_tr16_b64_v4i16((LAS s16x4*)(uintptr_t)a); }
DI bf16x8 cat8(s16x4 lo, s16x4 hi) { return __builtin_shufflevector(lo, hi, 0, 1, 2, 3, 4, 5, 6, 7); }
DI void glds16(const void* sbase, unsigned voff, unsigned lds_addr) {
  unsigned sv;
  asm volatile("s_mov_b32 %0, m0\n\ts_mov_b32 m0, %3\n\ts_nop 0\n\tglobal_load_lds_dwordx4 %1, %2\n\ts_mov_b32 m0, %0"
               : "=&s"(sv) : "v"(voff), "s"(sbase), "s"(lds_addr));
}
DI void touch_line(unsigned& sink, const void* sbase, unsigned voff) {
  asm volatile("global_load_dword %0, %1, %2" : "=v"(sink) : "v"(voff), "s"(sbase));
}
#define WAIT_VM(n) asm volatile("s_waitcnt vmcnt(%0)" ::"i"(n) : "memory")
#define WAIT_LGKM0() asm volatile("s_waitcnt lgkmcnt(0)" ::: "memory")
#define BARRIER() do { asm volatile("" ::: "memory"); __builtin_amdgcn_s_barrier(); asm volatile("" ::: "memory"); } while (0)
#define SBAR() __builtin_amdgcn_sched_barrier(0)
DI unsigned pinv(unsigned a) { asm volatile("" : "+v"(a)); return a; }

#define STAMP(kid, idx)

struct Params {
  const u16* x;
  const u16* wqkv;
  const u16* wo;
  const u16* w_in;
  const u16* w_sp;
  const u16* b_sp;
  const u16* w_out;
  u16* out;
  char* ws;
};

template <bool XQ>
DI int bswz(int k, int c) { return c ^ (((k >> 3) & 1) << 1) ^ (XQ ? (((k >> 1) & 1) << 2) : 0); }

constexpr int S1_STAGE = 16384 + 24576;
constexpr int S1_NBUF = 4;
constexpr int S1_LDS = S1_NBUF * S1_STAGE;
constexpr int S1_THREADS = 768;

template <int TYPE>
DI void stage1_body(const Params& p, int rg, int ct, unsigned lds0, int xcd, int jb) {
  constexpr int BN = TYPE == 2 ? 192 : 160;
  constexpr int CPR = BN / 8;
  constexpr int NF = BN / 32;
  constexpr int BI = BN / 32;
  constexpr int G = 4 + BI;
  constexpr int NT = D_ / 64;
  constexpr bool XQ = TYPE == 2;
  const int tid = TIDX, lane = tid & 63;
  const int wid = __builtin_amdgcn_readfirstlane(tid >> 6);
  const int fr = lane & 15, g = lane >> 4;
  const int wr = (wid >> 1) & 3, wc = wid & 1;

  LAS float* st_rs = (LAS float*)(uintptr_t)lds0;
  LAS float* st_sq = st_rs + 128;
  LAS float* st_cs = (LAS float*)(uintptr_t)(lds0 + 1024);
  const unsigned otile = lds0 + 4096;
  constexpr int OST = BN * 2 + 16;

  unsigned gofs[4] = {0, 0, 0, 0};
  if (TYPE == 0 && wid < 8) {
#pragma unroll
    for (int it = 0; it < 4; ++it) {
      int i = tid + it * 512;
      if (i >= 1680) i = 0;
      int hl = i / 420, w = i - hl * 420;
      int hi = hl & 1, ci, cj, q;
      if (w < 400) {
        int d = w / 20, e = w - d * 20;
        ci = hi * 20 + d; cj = 40 + hi * 20 + e;
        int pi = ci >> 4, pj = cj >> 4;
        q = pi == 0 ? pj - 2 : (pi == 1 ? pj : pj + 2);
      } else {
        ci = hi * 20 + (w - 400); cj = 0;
        q = 7 + (ci >> 4);
      }
      gofs[it] = pinv((hl >> 1) * 10240 + q * 1024 + ((((ci & 15) >> 2) * 16 + (cj & 15)) * 4 + (ci & 3)) * 4);
    }
  }

  if (wid >= 8) {
    const int lw = wid - 8;
    const u16* Bm = TYPE == 2 ? p.w_in : p.wqkv;
    constexpr int ldb = TYPE == 2 ? 2 * F_ : 3 * D_;
    unsigned aoff, boff[BI];
    {
      int row = lw * 8 + (lane >> 3), c = (lane & 7) ^ ((row >> 1) & 7);
      aoff = ((rg * 128 + row) * D_ + c * 8) * 2;
    }
#pragma unroll
    for (int i = 0; i < BI; ++i) {
      int slot = (i * 4 + lw) * 64 + lane;
      int k = slot / CPR, c = bswz<XQ>(k, slot - k * CPR);
      int col;
      if (TYPE == 0) col = c < 10 ? (D_ + ct * 80 + c * 8) : (2 * D_ + ct * 80 + (c - 10) * 8);
      else if (TYPE == 1) col = ct * 160 + c * 8;
      else col = F_ + ct * 192 + c * 8;
      boff[i] = (k * ldb + col) * 2;
    }
    auto issue = [&](int t) {
      unsigned base = lds0 + (t & 3) * S1_STAGE;
#pragma unroll
      for (int i = 0; i < 4; ++i) glds16(p.x + t * 64 + i * 32 * D_, aoff, base + (i * 4 + lw) * 1024);
#pragma unroll
      for (int i = 0; i < BI; ++i) glds16(Bm + (size_t)t * 64 * ldb, boff[i], base + 16384 + (i * 4 + lw) * 1024);
    };
    issue(0);
    issue(1);
    unsigned sk[5];
    WAIT_VM(G);
    BARRIER();
    issue(2);
    WAIT_VM(G);
    BARRIER();
#pragma unroll 1
    for (int t = 0; t < NT - 4; ++t) {
      issue(t + 3);
      WAIT_VM(G);
      BARRIER();
    }
    issue(NT - 1);
    WAIT_VM(G);
    BARRIER();
    WAIT_VM(0);
    BARRIER();
    if (TYPE != 0) {
      const int lt = lw * 64 + lane;
      const int i16 = (TYPE - 1) * 8 + (jb & 7);
      const int i128 = xcd * 16 + i16;
      touch_line(sk[0], p.w_sp, ((((xcd & 3) * 2 + (xcd >> 2)) * 16 + i16) * 512 + lt) * 128);
      touch_line(sk[1], p.w_sp, ((((xcd & 3) * 2 + (xcd >> 2)) * 16 + i16) * 512 + 256 + lt) * 128);
      int i = lt < 60 ? lt : 0;
      touch_line(sk[2], p.w_in, (((xcd & 3) * 160 + i16 * 10 + i / 6) * (2 * F_) + (xcd >> 2) * 384) * 2 + (i % 6) * 128);
      touch_line(sk[3], p.w_out, (i128 * 60 + (lt < 60 ? lt : 0)) * 128);
      touch_line(sk[4], p.wo, (i128 * 50 + (lt < 50 ? lt : 0)) * 128);
    }
    BARRIER();
    BARRIER();
    BARRIER();
    BARRIER();
    if (TYPE != 0) asm volatile("s_waitcnt vmcnt(0)" : "+v"(sk[0]), "+v"(sk[1]), "+v"(sk[2]), "+v"(sk[3]), "+v"(sk[4]) :: "memory");
    return;
  } else {
    unsigned abase[2], baddr[NF];
    {
      int x7 = (fr >> 1) & 7;
#pragma unroll
      for (int ks = 0; ks < 2; ++ks) abase[ks] = lds0 + (wr * 32 + wc * 16 + fr) * 128 + (((ks * 4 + g) ^ x7) << 4);
      int q = fr >> 2, pp = fr & 3;
#pragma unroll
      for (int n = 0; n < NF; ++n) {
        int c;
        if (TYPE == 0) {
          int w = 2 * n + (pp >> 1);
          c = w < 5 ? wc * 5 + w : 10 + wc * 5 + (w - 5);
        } else {
          c = 2 * (wc * NF + n) + (pp >> 1);
        }
        int k = 8 * g + q;
        baddr[n] = lds0 + 16384 + (k * CPR + bswz<XQ>(k, c)) * 16 + (pp & 1) * 8;
      }
    }
    const int am1 = wc ? -2048 : 2048;
    f32x4 acc[2][NF], cs[NF], rs, sq;
#pragma unroll
    for (int m = 0; m < 2; ++m)
#pragma unroll
      for (int n = 0; n < NF; ++n) acc[m][n] = f32x4{0, 0, 0, 0};
#pragma unroll
    for (int n = 0; n < NF; ++n) cs[n] = f32x4{0, 0, 0, 0};
    rs = f32x4{0, 0, 0, 0};
    sq = f32x4{0, 0, 0, 0};
    const short one = 0x3f80;
    bf16x8 ones = {one, one, one, one, one, one, one, one};
    asm volatile("" : "+v"(ones));

    bf16x8 af0[2], af1[2];
    s16x4 bl0[NF], bh0[NF], bl1[NF], bh1[NF];
    auto phase = [&](bf16x8 (&afc)[2], s16x4 (&blc)[NF], s16x4 (&bhc)[NF], bf16x8 (&afn)[2], s16x4 (&bln)[NF], s16x4 (&bhn)[NF],
                     unsigned sbn, int ksn, bool docs) {
      const unsigned a0 = pinv(abase[ksn] + sbn);
      unsigned ba = 0;
#pragma unroll
      for (int i = 0; i < 2 * NF + 2; ++i) {
        if (i < 2 * NF) {
          int m = i / NF, n = i % NF;
          bf16x8 bq = cat8(blc[n], bhc[n]);
          acc[m][n] = TYPE == 0 ? mfma16(afc[m], bq, acc[m][n]) : mfma16(bq, afc[m], acc[m][n]);
        } else if (i == 2 * NF) rs = mfma16(afc[0], ones, rs);
        else sq = mfma16(afc[0], afc[0], sq);
        if (i == 0) afn[0] = lds_b128(a0);
        else if (i <= 2 * NF) {
          int r = i - 1;
          if (r & 1) bhn[r >> 1] = lds_tr1(ba + (ksn * 32 + 4) * CPR * 16);
          else { ba = pinv(baddr[r >> 1] + sbn); bln[r >> 1] = lds_tr1(ba + ksn * 32 * CPR * 16); }
        } else afn[1] = lds_b128(a0 + am1);
        SBAR();
      }
      if (docs) {
#pragma unroll
        for (int n = 0; n < NF; ++n) cs[n] = mfma16(ones, cat8(blc[n], bhc[n]), cs[n]);
      }
      SBAR();
    };
    STAMP(0, 0);
    BARRIER();
    af0[0] = lds_b128(abase[0]);
    af0[1] = lds_b128(abase[0] + am1);
#pragma unroll
    for (int n = 0; n < NF; ++n) { bl0[n] = lds_tr1(baddr[n]); bh0[n] = lds_tr1(baddr[n] + 4 * CPR * 16); }
    phase(af0, bl0, bh0, af1, bl1, bh1, 0, 1, wr == 0);
    BARRIER();
    phase(af1, bl1, bh1, af0, bl0, bh0, S1_STAGE, 0, wr == 1);
    BARRIER();
#pragma unroll 1
    for (int t = 1; t < NT; ++t) {
      STAMP(0, 1 + t);
      phase(af0, bl0, bh0, af1, bl1, bh1, (t & 3) * S1_STAGE, 1, wr == ((2 * t) & 3));
      phase(af1, bl1, bh1, af0, bl0, bh0, ((t + 1) & 3) * S1_STAGE, 0, wr == ((2 * t + 1) & 3));
      if (t == NT - 1) WAIT_LGKM0();
      BARRIER();
    }
    STAMP(0, 100);
    {
      int r0 = wr * 32 + wc * 16;
      if (fr == 0) *(LAS f32x4*)(st_rs + r0 + g * 4) = rs;
      if ((fr >> 2) == g) {
        int r = fr & 3;
        st_sq[r0 + fr] = r == 0 ? sq[0] : r == 1 ? sq[1] : r == 2 ? sq[2] : sq[3];
      }
      if (g == 0) {
#pragma unroll
        for (int n = 0; n < NF; ++n) st_cs[wr * 192 + wc * NF * 16 + n * 16 + fr] = cs[n][0];
      }
    }
    WAIT_LGKM0();
    BARRIER();
    STAMP(0, 102);
    if (TYPE == 0) {
      float csv[NF];
#pragma unroll
      for (int n = 0; n < NF; ++n) {
        int c0 = wc * NF * 16 + n * 16 + fr;
        csv[n] = (st_cs[c0] + st_cs[192 + c0]) + (st_cs[384 + c0] + st_cs[576 + c0]);
      }
      bf16x8 xf[NF];
#pragma unroll
      for (int m = 0; m < 2; ++m) {
        int r0 = wr * 32 + (m ^ wc) * 16 + g * 4;
        f32x4 mean = *(LAS f32x4*)(st_rs + r0) * (1.f / D_);
        f32x4 ex2 = *(LAS f32x4*)(st_sq + r0) * (1.f / D_);
        f32x4 rstd, nm;
#pragma unroll
        for (int r = 0; r < 4; ++r) {
          rstd[r] = __builtin_amdgcn_rsqf(fmaxf(ex2[r] - mean[r] * mean[r], 0.f) + 1e-5f);
          nm[r] = -mean[r] * rstd[r];
        }
#pragma unroll
        for (int n = 0; n < NF; ++n) {
          f32x4 y = acc[m][n] * rstd + nm * csv[n];
          bool isk = (n * 16 + fr) < 40;
          float v[4];
#pragma unroll
          for (int r = 0; r < 4; ++r) v[r] = n < 2 ? phi_f(y[r]) : (n == 2 ? (isk ? phi_f(y[r]) : y[r]) : y[r]);
          u32x4 t4 = __builtin_bit_cast(u32x4, xf[n]);
          t4[m * 2] = pack_bf16(v[0], v[1]);
          t4[m * 2 + 1] = pack_bf16(v[2], v[3]);
          xf[n] = __builtin_bit_cast(bf16x8, t4);
        }
      }
      SBAR();
      STAMP(0, 104);
      constexpr int PI[7] = {0, 0, 1, 1, 1, 2, 2};
      constexpr int PJ[7] = {2, 3, 2, 3, 4, 3, 4};
      f32x4 dd[10];
#pragma unroll
      for (int q = 0; q < 7; ++q) dd[q] = mfma16(xf[PI[q]], xf[PJ[q]], f32x4{0, 0, 0, 0});
#pragma unroll
      for (int i = 0; i < 3; ++i) dd[7 + i] = mfma16(xf[i], ones, f32x4{0, 0, 0, 0});
      SBAR();
#pragma unroll
      for (int q = 0; q < 10; ++q) *(LAS f32x4*)(uintptr_t)(otile + wid * 10240 + q * 1024 + lane * 16) = dd[q];
    } else {
      f32x4 csv[NF];
#pragma unroll
      for (int n = 0; n < NF; ++n) {
        int c0 = wc * NF * 16 + n * 16 + g * 4;
        csv[n] = (*(LAS f32x4*)(st_cs + c0) + *(LAS f32x4*)(st_cs + 192 + c0)) + (*(LAS f32x4*)(st_cs + 384 + c0) + *(LAS f32x4*)(st_cs + 576 + c0));
      }
#pragma unroll
      for (int m = 0; m < 2; ++m) {
        int rl = wr * 32 + (m ^ wc) * 16 + fr;
        float mean = st_rs[rl] * (1.f / D_);
        float ex2 = st_sq[rl] * (1.f / D_);
        float rstd = __builtin_amdgcn_rsqf(fmaxf(ex2 - mean * mean, 0.f) + 1e-5f);
        float nm = -mean * rstd;
        if (TYPE == 2 && ct == 0 && m == 0 && g == 0) *(f32x2*)((float*)(p.ws + WS_XST) + (size_t)(rg * 128 + rl) * 2) = f32x2{nm, rstd};
        unsigned dst = otile + rl * OST + (wc * NF * 16 + g * 4) * 2;
#pragma unroll
        for (int n = 0; n < NF; ++n) {
          f32x4 y = acc[m][n] * rstd + csv[n] * nm;
          *(LAS u32x2*)(uintptr_t)(dst + n * 32) = u32x2{pack_bf16(y[0], y[1]), pack_bf16(y[2], y[3])};
        }
      }
    }
    STAMP(0, 105);
  }
  WAIT_LGKM0();
  BARRIER();
  STAMP(0, 106);
  if (TYPE == 0) {
    float* kvp = (float*)(p.ws + WS_KVP) + ((size_t)rg * 32 + ct * 4) * 420;
#pragma unroll
    for (int it = 0; it < 4; ++it) {
      int i = tid + it * 512;
      if (i < 1680) {
        unsigned a = otile + gofs[it];
        float v = (*(LAS float*)(uintptr_t)(a) + *(LAS float*)(uintptr_t)(a + 2 * 10240)) +
                  (*(LAS float*)(uintptr_t)(a + 4 * 10240) + *(LAS float*)(uintptr_t)(a + 6 * 10240));
        kvp[i] = v;
      }
    }
  } else {
    u16* dstm = TYPE == 1 ? (u16*)(p.ws + WS_Q) + (size_t)rg * 128 * D_ + ct * 160
                          : (u16*)(p.ws + WS_Y2) + (size_t)rg * 128 * F_ + ct * 192;
    constexpr int ldo = TYPE == 1 ? D_ : F_;
    for (int idx = tid; idx < 128 * CPR; idx += 512) {
      int row = idx / CPR, c = idx - row * CPR;
      u32x4 v = *(LAS u32x4*)(uintptr_t)(otile + row * OST + c * 16);
      *(u32x4*)(dstm + (size_t)row * ldo + c * 8) = v;
    }
  }
}

__global__ __launch_bounds__(S1_THREADS, 3) void stage1_kernel(Params p) {
  extern __shared__ __attribute__((aligned(16))) char shm[];
  unsigned lds0 = (unsigned)(uintptr_t)(LAS char*)shm;
  int b = BIDX, xcd = b & 7, j = b >> 3;
  int rg = (xcd & 1) * 8 + (j & 7);
  int cq = xcd >> 1, ci = j >> 3;
  STAMP(0, 200);
  if (b == 0) {
    for (int i = TIDX; i < 64 * 32; i += S1_THREADS) ((unsigned*)(p.ws + WS_FLAGS))[i] = 0u;
  }
  if (ci < 2) stage1_body<0>(p, rg, cq * 2 + ci, lds0, xcd, j);
  else if (ci == 2) stage1_body<1>(p, rg, cq, lds0, xcd, j);
  else stage1_body<2>(p, rg, cq, lds0, xcd, j);
  STAMP(0, 201);
}

constexpr int K2_SLOT = 8192 + 12288;
constexpr int K2_NS = 7;
constexpr int K2_SCR = K2_NS * K2_SLOT;
constexpr int K2_LDS = K2_SCR + 20480;
constexpr int K2_MID = (K2_NS - 1) * K2_SLOT;
constexpr int K2_THREADS = 1024;
constexpr int T_G = 10, T_A = 42, T_B = 52, T_END = 64;
constexpr unsigned SPIN_CAP = 1u << 22;

typedef __attribute__((address_space(1))) unsigned gu32;
DI void* uni_ptr(const void* q) {
  unsigned long long a = (unsigned long long)q;
  unsigned lo = __builtin_amdgcn_readfirstlane((unsigned)a), hi = __builtin_amdgcn_readfirstlane((unsigned)(a >> 32));
  return (void*)(((unsigned long long)hi << 32) | lo);
}
DI void store16_sc1(void* sbase, unsigned voff, u32x4 v) {
  sbase = uni_ptr(sbase);
  asm volatile("s_nop 4\n\tglobal_store_dwordx4 %0, %1, %2 sc1\n\ts_nop 1" ::"v"(voff), "v"(v), "s"(sbase) : "memory");
}
DI void store8_sc1(void* sbase, unsigned voff, u32x2 v) {
  asm volatile("global_store_dwordx2 %0, %1, %2 sc1\n\ts_nop 1" ::"v"(voff), "v"(v), "s"(sbase) : "memory");
}
DI void glds16_sc1(const void* sbase, unsigned voff, unsigned lds_addr) {
  unsigned sv;
  asm volatile("s_mov_b32 %0, m0\n\ts_mov_b32 m0, %3\n\ts_nop 0\n\tglobal_load_lds_dwordx4 %1, %2 sc1\n\ts_mov_b32 m0, %0"
               : "=&s"(sv) : "v"(voff), "s"(sbase), "s"(lds_addr));
}
DI void wait_flags(unsigned* flags, int nw, unsigned target, int lane) {
  gu32* fp = (gu32*)(flags + (lane < nw ? lane : 0) * 32);
  for (unsigned spins = 0; spins < SPIN_CAP; ++spins) {
    unsigned v = __hip_atomic_load(fp, __ATOMIC_RELAXED, __HIP_MEMORY_SCOPE_AGENT);
    if (__all(v >= target)) break;
    __builtin_amdgcn_s_sleep(1);
  }
  __builtin_amdgcn_fence(__ATOMIC_ACQUIRE, "wavefront");
}

__global__ __launch_bounds__(K2_THREADS, 4) void stage2_kernel(Params p) {
  extern __shared__ __attribute__((aligned(16))) char shm[];
  const unsigned lds0 = (unsigned)(uintptr_t)(LAS char*)shm;
  const int b = BIDX, xcd = b & 7, j = b >> 3;
  const int rg = (xcd & 3) * 8 + (j & 7);
  const int cb = (xcd >> 2) * 4 + (j >> 3);
  const int rg3 = xcd * 4 + (j & 3), cb3 = j >> 2;
  const int tid = TIDX, lane = tid & 63;
  const int wid = __builtin_amdgcn_readfirstlane(tid >> 6);
  const int fr = lane & 15, g = lane >> 4;
  unsigned* flags = (unsigned*)(p.ws + WS_FLAGS);
  constexpr int CPR = 12;
  STAMP(1, 200);

  if (wid >= 12) {
    const int st = tid - 768, sw = wid - 12;
    const int mcb = b & 7;
    const unsigned scr = lds0 + K2_SCR;
    LAS float* zt = (LAS float*)(uintptr_t)(scr + 18432);
#define SB(n) do { _Pragma("unroll 1") for (int i_ = 0; i_ < (n); ++i_) BARRIER(); } while (0)
#define FLAG_ADD_S(off) do { if (st == 0) __hip_atomic_fetch_add((gu32*)(flags + (off)), 1u, __ATOMIC_RELAXED, __HIP_MEMORY_SCOPE_AGENT); } while (0)
    u32x4 w0, w1;
    u32x2 qw[5];
    {
      const u16* wsrc = b < 128 ? p.wo + (size_t)(b >> 3) * 40 * D_ : p.w_out + (size_t)((b - 128) >> 3) * 48 * D_;
      const int nch = b < 128 ? 400 : 480;
      int i1 = st + 256 < nch ? st + 256 : 0;
      w0 = *(const u32x4*)(wsrc + (st / 10) * D_ + mcb * 80 + (st % 10) * 8);
      w1 = *(const u32x4*)(wsrc + (i1 / 10) * D_ + mcb * 80 + (i1 % 10) * 8);
      const u16* qsrc = (const u16*)(p.ws + WS_Q) + (size_t)((b >> 1) * 16 + (st >> 4)) * D_ + (b & 1) * 320 + (st & 15) * 20;
#pragma unroll
      for (int q = 0; q < 5; ++q) qw[q] = *(const u32x2*)(qsrc + q * 4);
    }
    SB(3);
    float pq[20];
#pragma unroll
    for (int q = 0; q < 3; ++q) {
      pq[q * 4] = phi_f(bf_lo(qw[q][0])); pq[q * 4 + 1] = phi_f(bf_hi(qw[q][0]));
      pq[q * 4 + 2] = phi_f(bf_lo(qw[q][1])); pq[q * 4 + 3] = phi_f(bf_hi(qw[q][1]));
    }
    SBAR();
    SB(1);
#pragma unroll
    for (int q = 3; q < 5; ++q) {
      pq[q * 4] = phi_f(bf_lo(qw[q][0])); pq[q * 4 + 1] = phi_f(bf_hi(qw[q][0]));
      pq[q * 4 + 2] = phi_f(bf_lo(qw[q][1])); pq[q * 4 + 3] = phi_f(bf_hi(qw[q][1]));
    }
    *(LAS u32x4*)(uintptr_t)(scr + 8192 + st * 16) = w0;
    if (st + 256 < (b < 128 ? 400 : 480)) *(LAS u32x4*)(uintptr_t)(scr + 8192 + (st + 256) * 16) = w1;
    if (b < 128) {
      *(LAS u32x4*)(uintptr_t)(scr + st * 16) = u32x4{0, 0, 0, 0};
      *(LAS u32x4*)(uintptr_t)(scr + 4096 + st * 16) = u32x4{0, 0, 0, 0};
      if (st < 240) *(LAS u32x4*)(uintptr_t)(scr + 8192 + 6400 + st * 16) = u32x4{0, 0, 0, 0};
    }
    WAIT_LGKM0();
    SB(3);
    if (sw == 0) wait_flags(flags, 16, 16u, lane);
    SB(1);
    u32x4 tb0, tb1;
    {
      unsigned o0 = (((b < 128 ? b >> 3 : 0) * 2 + (st < 200 ? st / 100 : 0)) * 420 + (st % 100) * 4) * 4;
      int i4 = (st < 80 ? st : 0) * 4;
      unsigned o1 = (((b & 1) * 16 + i4 / 20) * 420 + 400 + i4 % 20) * 4;
      asm volatile("global_load_dwordx4 %0, %1, %2 sc1" : "=v"(tb0) : "v"(o0), "s"(p.ws + WS_KVR));
      asm volatile("global_load_dwordx4 %0, %1, %2 sc1" : "=v"(tb1) : "v"(o1), "s"(p.ws + WS_KVR));
    }
    const int q4 = fr >> 2, pp = fr & 3;
    if (b >= 128) {
#pragma unroll
      for (int it = 0; it < 2; ++it) {
        int blk = (it * 4 + sw) * 4 + g;
        int bb = blk < 30 ? blk : 0;
        int kc = bb / 5, nf = bb - kc * 5;
        unsigned wa = scr + 8192 + (kc * 8 + q4) * 160 + nf * 32 + pp * 8;
        bf16x8 v = cat8(lds_tr1(wa), lds_tr1(wa + 4 * 160));
        if (blk < 30) store16_sc1(p.ws + WS_B3, ((mcb * 176 + 80 + ((b - 128) >> 3) * 6 + kc) * 80 + nf * 16 + fr) * 16, __builtin_bit_cast(u32x4, v));
      }
    }
    SB(5);
    asm volatile("s_waitcnt vmcnt(0)" : "+v"(tb0), "+v"(tb1) :: "memory");
    if (st < 80) *(LAS u32x4*)(zt + st * 4) = tb1;
    if (b < 128 && st < 200) {
      int hh = st / 100, w = (st - hh * 100) * 4, dl = w / 20, el = w - dl * 20;
      int d = hh * 20 + dl, e = hh * 20 + el;
      int row = (d >> 5) * 32 + ((d >> 2) & 1) * 16 + ((d >> 3) & 3) * 4 + (d & 3);
      f32x4 kf = __builtin_bit_cast(f32x4, tb0);
      unsigned a = scr + row * 128 + ((((e >> 3) ^ ((row >> 1) & 7)) << 4) | ((e & 4) << 1));
      *(LAS u32x2*)(uintptr_t)a = u32x2{pack_bf16(kf[0], kf[1]), pack_bf16(kf[2], kf[3])};
    }
    WAIT_LGKM0();
    SB(1);
    {
      float den = 0.f;
#pragma unroll
      for (int q = 0; q < 5; ++q) {
        f32x4 zz = *(LAS f32x4*)(zt + (st & 15) * 20 + q * 4);
        den += pq[q * 4] * zz[0] + pq[q * 4 + 1] * zz[1] + pq[q * 4 + 2] * zz[2] + pq[q * 4 + 3] * zz[3];
      }
      float inv = 1.f / (den + 1e-6f);
#pragma unroll
      for (int q = 0; q < 5; ++q)
        qw[q] = u32x2{pack_bf16(pq[q * 4] * inv, pq[q * 4 + 1] * inv), pack_bf16(pq[q * 4 + 2] * inv, pq[q * 4 + 3] * inv)};
    }
    SBAR();
    SB(1);
#pragma unroll
    for (int it = 0; it < 3; ++it) {
      if (b < 128) {
        int unit = sw + it * 4;
        int u = unit < 10 ? unit : 0;
        int db = u / 5, nf = u - db * 5;
        f32x4 d0 = {0, 0, 0, 0}, d1 = {0, 0, 0, 0};
#pragma unroll
        for (int ks = 0; ks < 2; ++ks) {
          unsigned wa = scr + 8192 + (ks * 32 + g * 8 + q4) * 160 + nf * 32 + pp * 8;
          bf16x8 bw = cat8(lds_tr1(wa), lds_tr1(wa + 4 * 160));
          unsigned aa = scr + (db * 32 + fr) * 128 + (((ks * 4 + g) ^ ((fr >> 1) & 7)) << 4);
          d0 = mfma16(lds_b128(aa), bw, d0);
          d1 = mfma16(lds_b128(aa + 2048), bw, d1);
        }
        int kc = db * 4 + g;
        if (unit < 10 && kc < 5) {
          u32x4 o = {pack_bf16(d0[0], d0[1]), pack_bf16(d0[2], d0[3]), pack_bf16(d1[0], d1[1]), pack_bf16(d1[2], d1[3])};
          store16_sc1(p.ws + WS_B3, ((mcb * 176 + (b >> 3) * 5 + kc) * 80 + nf * 16 + fr) * 16, o);
        }
      }
      WAIT_LGKM0();
      SB(1);
    }
    constexpr int PST = 640 + 16;
    {
      unsigned a = scr + (st >> 4) * PST + (st & 15) * 40;
#pragma unroll
      for (int q = 0; q < 5; ++q) *(LAS u32x2*)(uintptr_t)(a + q * 8) = qw[q];
    }
    WAIT_LGKM0();
    SB(1);
    {
      const int prg = b >> 3, r0 = ((b >> 1) & 3) * 16;
#pragma unroll
      for (int it = 0; it < 3; ++it) {
        int idx = st + it * 256;
        if (idx < 640) {
          int ktl = idx >> 7, row = (idx >> 3) & 15, cp = idx & 7;
          int c = cp ^ (((r0 + row) >> 1) & 7);
          u32x4 v = *(LAS u32x4*)(uintptr_t)(scr + row * PST + (ktl * 8 + c) * 16);
          store16_sc1(p.ws + WS_UA, ((prg * 22 + (b & 1) * 5 + ktl) * 64 + r0 + row) * 128 + cp * 16, v);
        }
      }
    }
    SB(5);
    WAIT_VM(0);
    SB(1);
    FLAG_ADD_S(512 + (b & 15) * 32);
    SB(12);
    if (sw == 0) wait_flags(flags + 512, 16, 16u, lane);
    unsigned ul[3], ug[3];
    {
      constexpr int OST = 96 * 2 + 16;
#pragma unroll
      for (int it = 0; it < 3; ++it) {
        int idx = st + it * 256;
        int row = idx / CPR, c = idx - row * CPR;
        int cg = cb * 12 + c;
        ul[it] = pinv(scr + row * OST + c * 16);
        ug[it] = pinv(((rg * 22 + 10 + (cg >> 3)) * 64 + row) * 128 + (((cg & 7) ^ ((row >> 1) & 7)) << 4));
      }
    }
    SB(7);
    {
      u32x4 v[3];
#pragma unroll
      for (int it = 0; it < 3; ++it) v[it] = *(LAS u32x4*)(uintptr_t)ul[it];
#pragma unroll
      for (int it = 0; it < 3; ++it) store16_sc1(p.ws + WS_UA, ug[it], v[it]);
    }
    SB(2);
    WAIT_VM(0);
    SB(1);
    FLAG_ADD_S(1024 + rg * 32);
    SB(6);
    if (sw == 0) wait_flags(flags + 1024 + rg3 * 32, 1, 8u, lane);
    SB(1);
  } else if (wid >= 8) {
    __builtin_amdgcn_s_setprio(3);
    const int lw = wid - 8, lt = tid - 512;
    unsigned aoff1, aoff2, boff1[3];
    {
      int row = lw * 8 + (lane >> 3), c = (lane & 7) ^ ((row >> 1) & 7);
      aoff1 = ((rg * 64 + row) * D_ + c * 8) * 2;
      aoff2 = ((rg * 64 + row) * S_ + c * 8) * 2;
    }
#pragma unroll
    for (int i = 0; i < 3; ++i) {
      int slot = i * 256 + lt;
      int k = slot / CPR;
      boff1[i] = (k * (2 * F_) + cb * 96 + bswz<false>(k, slot - k * CPR) * 8) * 2;
    }
    const unsigned boff2 = lt * 16;
    const u16* zpanel = (const u16*)(p.ws + WS_Z2N) + (size_t)cb * S_ * 96;
    const u16* bpanel = (const u16*)(p.ws + WS_B3) + (size_t)cb3 * KA_ * 80;
    const u16* ua = (const u16*)(p.ws + WS_UA) + (size_t)rg3 * 22 * 4096;
    const bool b3 = lw < 2;
    const unsigned lbase = lds0 + lw * 1024;
    auto ld_z1_a = [&](int t, int sl) {
#pragma unroll
      for (int i = 0; i < 2; ++i) glds16(p.x + t * 64 + i * 32 * D_, aoff1, lbase + sl * K2_SLOT + i * 4096);
    };
    auto ld_z1_b = [&](int t, int sl) {
#pragma unroll
      for (int i = 0; i < 3; ++i) glds16(p.w_in + (size_t)t * 64 * (2 * F_), boff1[i], lbase + sl * K2_SLOT + 8192 + i * 4096);
    };
    auto ld_g_a = [&](int t, int sl) {
#pragma unroll
      for (int i = 0; i < 2; ++i) glds16(p.w_sp + t * 64 + i * 32 * S_, aoff2, lbase + sl * K2_SLOT + i * 4096);
    };
    auto ld_g_b = [&](int t, int sl) {
#pragma unroll
      for (int i = 0; i < 3; ++i) glds16_sc1(zpanel + (size_t)t * 64 * 96 + i * 2048, boff2, lbase + sl * K2_SLOT + 8192 + i * 4096);
    };
    auto ld_o_a = [&](int t, int sl) {
#pragma unroll
      for (int i = 0; i < 2; ++i) glds16_sc1(ua + t * 4096 + i * 2048, boff2, lbase + sl * K2_SLOT + i * 4096);
    };
    auto ld_o_b = [&](int t, int sl) {
#pragma unroll
      for (int i = 0; i < 2; ++i) glds16_sc1(bpanel + (size_t)t * 64 * 80 + i * 2048, boff2, lbase + sl * K2_SLOT + 8192 + i * 4096);
      if (b3) glds16_sc1(bpanel + (size_t)t * 64 * 80 + 4096, boff2, lbase + sl * K2_SLOT + 8192 + 8192);
    };
    int sl = 0;
    auto nx = [&]() { sl = sl == K2_NS - 1 ? 0 : sl + 1; };
#pragma unroll 1
    for (int t = 0; t < 2; ++t) { ld_z1_a(t, sl); ld_z1_b(t, sl); nx(); }
    WAIT_VM(5); BARRIER();
#pragma unroll 1
    for (int t = 2; t < 4; ++t) { ld_z1_a(t, sl); ld_z1_b(t, sl); nx(); }
    WAIT_VM(10); BARRIER();
#pragma unroll 1
    for (int t = 4; t < 6; ++t) { ld_z1_a(t, sl); ld_z1_b(t, sl); nx(); }
#pragma unroll 1
    for (int t = 6; t < 10; ++t) { ld_z1_a(t, sl); ld_z1_b(t, sl); nx(); WAIT_VM(20); BARRIER(); }
    ld_g_a(0, 3); WAIT_VM(17); BARRIER();
    ld_g_a(1, 4); WAIT_VM(14); BARRIER();
    ld_g_b(0, 3); ld_g_b(1, 4);
    sl = 5;
#pragma unroll 1
    for (int t = 2; t < 32; ++t) {
      ld_g_a(t, sl); ld_g_b(t, sl); nx();
      if (t == 4) WAIT_VM(18); else WAIT_VM(20);
      BARRIER();
    }
    ld_o_a(0, sl); ld_o_b(0, sl); nx(); if (b3) WAIT_VM(20); else WAIT_VM(19); BARRIER();
    ld_o_a(1, sl); ld_o_b(1, sl); nx(); if (b3) WAIT_VM(20); else WAIT_VM(18); BARRIER();
    ld_o_a(2, sl); ld_o_b(2, sl); nx(); if (b3) WAIT_VM(20); else WAIT_VM(17); BARRIER();
#pragma unroll 1
    for (int t = 3; t < 10; ++t) {
      ld_o_a(t, sl); ld_o_b(t, sl); nx();
      if (b3) WAIT_VM(20); else WAIT_VM(16);
      BARRIER();
    }
    const int sl52 = sl;
    ld_o_b(10, sl); nx(); if (b3) WAIT_VM(18); else WAIT_VM(14); BARRIER();
    ld_o_b(11, sl); nx(); if (b3) WAIT_VM(16); else WAIT_VM(12); BARRIER();
    ld_o_b(12, sl); nx(); if (b3) WAIT_VM(14); else WAIT_VM(10); BARRIER();
    ld_o_b(13, sl); nx(); if (b3) WAIT_VM(12); else WAIT_VM(8); BARRIER();
    ld_o_b(14, sl); nx(); BARRIER();
    ld_o_b(15, sl); nx(); BARRIER();
    {
      int s2 = sl52;
#pragma unroll 1
      for (int t = 10; t < 16; ++t) { ld_o_a(t, s2); s2 = s2 == K2_NS - 1 ? 0 : s2 + 1; }
    }
    WAIT_VM(10); BARRIER();
    WAIT_VM(8); BARRIER();
    ld_o_a(16, sl); ld_o_b(16, sl); nx(); if (b3) WAIT_VM(11); else WAIT_VM(10); BARRIER();
    ld_o_a(17, sl); ld_o_b(17, sl); nx(); if (b3) WAIT_VM(14); else WAIT_VM(12); BARRIER();
    ld_o_a(18, sl); ld_o_b(18, sl); nx(); if (b3) WAIT_VM(17); else WAIT_VM(14); BARRIER();
#pragma unroll 1
    for (int t = 19; t < 22; ++t) {
      ld_o_a(t, sl); ld_o_b(t, sl); nx();
      if (b3) WAIT_VM(20); else WAIT_VM(16);
      BARRIER();
    }
    if (b3) WAIT_VM(15); else WAIT_VM(12); BARRIER();
    if (b3) WAIT_VM(10); else WAIT_VM(8); BARRIER();
    if (b3) WAIT_VM(5); else WAIT_VM(4); BARRIER();
    WAIT_VM(0); BARRIER();
    BARRIER();
    BARRIER();
  } else {
    const int wr = wid >> 1, wc = wid & 1;
    const int kh = wid >> 2, wm = wid & 3;
    const int mcb = b & 7;
    const unsigned scr = lds0 + K2_SCR;
    LAS float* sx = (LAS float*)(uintptr_t)scr;
    LAS float* cst = (LAS float*)(uintptr_t)(scr + 16384);
    LAS float* krp = (LAS float*)(uintptr_t)(scr + 16384);
    const int urow = wr * 16 + fr;

    constexpr int ZST = 1536 + 32;
    u32x2 yv[3];
    u32x4 tb0;
    {
      unsigned oy = ((b * 8 + wid) * F_ + lane * 4) * 2;
#pragma unroll
      for (int i = 0; i < 3; ++i) asm volatile("global_load_dwordx2 %0, %1, %2" : "=v"(yv[i]) : "v"(oy + i * 512), "s"(p.ws + WS_Y2));
      unsigned ok = (((tid >> 4) & 15) * 13440 + (b < 210 ? b : 0) * 64 + (tid & 15) * 4) * 4;
      asm volatile("global_load_dwordx4 %0, %1, %2" : "=v"(tb0) : "v"(ok), "s"(p.ws + WS_KVP));
    }
    auto parta_compute = [&]() {
      asm volatile("s_waitcnt vmcnt(0)" : "+v"(yv[0]), "+v"(yv[1]), "+v"(yv[2]), "+v"(tb0) :: "memory");
      float z[12];
#pragma unroll
      for (int i = 0; i < 3; ++i)
#pragma unroll
        for (int q = 0; q < 2; ++q) {
          unsigned w = pack_bf16(gelu_f(bf_lo(yv[i][q])), gelu_f(bf_hi(yv[i][q])));
          z[i * 4 + q * 2] = bf_lo(w);
          z[i * 4 + q * 2 + 1] = bf_hi(w);
        }
      float s = 0.f;
#pragma unroll
      for (int i = 0; i < 12; ++i) s += z[i];
      s = wave_sum(s);
      float mean = s * (1.f / F_), v2 = 0.f;
#pragma unroll
      for (int i = 0; i < 12; ++i) { z[i] -= mean; v2 = __builtin_fmaf(z[i], z[i], v2); }
      v2 = wave_sum(v2);
      float rstd = __builtin_amdgcn_rsqf(v2 * (1.f / F_) + 1e-5f);
#pragma unroll
      for (int i = 0; i < 3; ++i)
        *(LAS u32x2*)(uintptr_t)(scr + wid * ZST + (i * 256 + lane * 4) * 2) =
            u32x2{pack_bf16(z[i * 4] * rstd, z[i * 4 + 1] * rstd), pack_bf16(z[i * 4 + 2] * rstd, z[i * 4 + 3] * rstd)};
      if (wid < 4) *(LAS u32x4*)(krp + tid * 4) = tb0;
      WAIT_LGKM0();
    };
    auto parta_store = [&]() {
#pragma unroll
      for (int it = 0; it < 2; ++it) {
        int grp64 = it ? 8 + (wid & 3) : wid;
        int q = fr >> 2, pp = fr & 3;
        unsigned a = scr + q * ZST + (grp64 * 64 + g * 16 + pp * 4) * 2;
        bf16x8 v = cat8(lds_tr1(a), lds_tr1(a + 4 * ZST));
        int f = grp64 * 64 + lane, c = f / 96, n = f - c * 96;
        if (it == 0 || wid < 4) store16_sc1(p.ws + WS_Z2N, ((c * 256 + b) * 96 + n) * 16, __builtin_bit_cast(u32x4, v));
      }
      if (tid >= 448 && tid < 464 && b < 210) {
        int v = tid - 448;
        f32x4 t4 = {0, 0, 0, 0};
#pragma unroll
        for (int q = 0; q < 16; ++q) t4 += *(LAS f32x4*)(krp + q * 64 + v * 4);
        store16_sc1(p.ws + WS_KVR, (b * 64 + v * 4) * 4, __builtin_bit_cast(u32x4, t4));
      }
    };

    unsigned abase[2], baddr1[3], baddr2, abase3, baddr3;
    {
      int x7 = (fr >> 1) & 7;
#pragma unroll
      for (int ks = 0; ks < 2; ++ks) abase[ks] = lds0 + (wr * 16 + fr) * 128 + (((ks * 4 + g) ^ x7) << 4);
      int q = fr >> 2, pp = fr & 3;
#pragma unroll
      for (int n = 0; n < 3; ++n) {
        int k = 8 * g + q;
        baddr1[n] = lds0 + 8192 + (k * CPR + bswz<false>(k, 2 * (wc * 3 + n) + (pp >> 1))) * 16 + (pp & 1) * 8;
      }
      baddr2 = lds0 + 8192 + (g * 96 + wc * 48 + fr) * 16;
      abase3 = lds0 + (wm * 16 + fr) * 128 + (((kh * 4 + g) ^ x7) << 4);
      baddr3 = lds0 + 8192 + ((kh * 4 + g) * 80 + fr) * 16;
    }
    f32x4 acc1[3], acc2[3], cs[3], acc3[5];
#pragma unroll
    for (int n = 0; n < 3; ++n) { acc1[n] = f32x4{0, 0, 0, 0}; acc2[n] = f32x4{0, 0, 0, 0}; cs[n] = f32x4{0, 0, 0, 0}; }
#pragma unroll
    for (int n = 0; n < 5; ++n) acc3[n] = f32x4{0, 0, 0, 0};
    const short one = 0x3f80;
    bf16x8 ones = {one, one, one, one, one, one, one, one};
    asm volatile("" : "+v"(ones));

    bf16x8 fA[8], fB[8];
    unsigned slot = 0;
    int z1t = 0;
    auto adv = [&]() { slot = slot == (K2_NS - 1) * K2_SLOT ? 0 : slot + K2_SLOT; };
    auto ldf = [&](bf16x8 (&f)[8], int mode, int i, unsigned& tmp) {
      if (mode == 2) {
        if (i == 0) f[0] = lds_b128(pinv(abase3 + slot));
        else if (i < 6) { if (i == 1) tmp = pinv(baddr3 + slot); f[1 + i] = lds_b128(tmp + (i - 1) * 256); }
      } else {
        if (i == 0 || i == 4) f[i >> 2] = lds_b128(pinv(abase[i >> 2] + slot));
        else {
          int ks = i >> 2, n = (i & 3) - 1;
          if (mode == 0) {
            unsigned a = pinv(baddr1[n] + slot) + ks * 32 * CPR * 16;
            f[2 + ks * 3 + n] = cat8(lds_tr1(a), lds_tr1(a + 4 * CPR * 16));
          } else {
            if (i == 1) tmp = pinv(baddr2 + slot);
            f[2 + ks * 3 + n] = lds_b128(tmp + ks * 4 * 96 * 16 + n * 256);
          }
        }
      }
    };
    auto iter = [&](bf16x8 (&fc)[8], bf16x8 (&fn)[8], auto mc, auto mn) {
      constexpr int MC = decltype(mc)::value, MN = decltype(mn)::value;
      unsigned tmp = 0;
#pragma unroll
      for (int i = 0; i < 8; ++i) {
        if (MC == 2) {
          if (i < 5) acc3[i] = mfma16(fc[2 + i], fc[0], acc3[i]);
        } else if (i < 6) {
          int ks = i / 3, n = i % 3;
          if (MC == 0) acc1[n] = mfma16(fc[2 + i], fc[ks], acc1[n]);
          else acc2[n] = mfma16(fc[2 + i], fc[ks], acc2[n]);
        }
        if (MN < 3) ldf(fn, MN, i, tmp);
        SBAR();
      }
      if (MC == 0) {
        if (wr == (z1t & 3)) {
#pragma unroll
          for (int i = 0; i < 6; ++i) cs[i % 3] = mfma16(fc[2 + i], ones, cs[i % 3]);
        }
        ++z1t;
        SBAR();
      }
      if (MN < 3) adv();
    };
    using I0 = std::integral_constant<int, 0>;
    using I1 = std::integral_constant<int, 1>;
    using I2 = std::integral_constant<int, 2>;
    using I3 = std::integral_constant<int, 3>;
#define FLAG_ADD(off) do { if (tid == 0) __hip_atomic_fetch_add((gu32*)(flags + (off)), 1u, __ATOMIC_RELAXED, __HIP_MEMORY_SCOPE_AGENT); } while (0)
    const f32x2 xs = *(const f32x2*)((const float*)(p.ws + WS_XST) + (size_t)(rg * 64 + urow) * 2);
    const float bias = bf_lo(p.b_sp[rg * 64 + urow]);
    parta_compute();
    STAMP(1, 209);
    BARRIER();
    STAMP(1, 0);
    parta_store();
    {
      unsigned tmp = 0;
#pragma unroll
      for (int i = 0; i < 8; ++i) ldf(fA, 0, i, tmp);
      adv();
    }
    BARRIER();
    iter(fA, fB, I0{}, I0{}); WAIT_VM(0); BARRIER();
    FLAG_ADD((b & 15) * 32);
    STAMP(1, 1);
    iter(fB, fA, I0{}, I0{}); BARRIER();
    iter(fA, fB, I0{}, I0{}); BARRIER();
    iter(fB, fA, I0{}, I0{}); BARRIER();
    STAMP(1, 4);
    iter(fA, fB, I0{}, I0{}); BARRIER();
    iter(fB, fA, I0{}, I0{}); BARRIER();
    STAMP(1, 6);
    iter(fA, fB, I0{}, I0{}); BARRIER();
    iter(fB, fA, I0{}, I0{}); BARRIER();
    iter(fA, fB, I0{}, I0{}); BARRIER();
    iter(fB, fA, I0{}, I1{}); BARRIER();
    STAMP(1, 10);
#pragma unroll 1
    for (int t = 10; t < 18; t += 2) {
      iter(fA, fB, I1{}, I1{}); BARRIER();
      iter(fB, fA, I1{}, I1{}); BARRIER();
    }
    STAMP(1, 18);
    iter(fA, fB, I1{}, I1{});
    if (fr == 0) {
#pragma unroll
      for (int n = 0; n < 3; ++n) *(LAS f32x4*)(cst + wr * 96 + wc * 48 + n * 16 + g * 4) = cs[n];
    }
    WAIT_LGKM0();
    BARRIER();
    auto z1act = [&](int n) {
      SBAR();
      int c0 = wc * 48 + n * 16 + g * 4;
      f32x4 c4 = (*(LAS f32x4*)(cst + c0) + *(LAS f32x4*)(cst + 96 + c0)) + (*(LAS f32x4*)(cst + 192 + c0) + *(LAS f32x4*)(cst + 288 + c0));
#pragma unroll
      for (int r = 0; r < 4; ++r) acc1[n][r] = gelu_f(__builtin_fmaf(acc1[n][r], xs[1], c4[r] * xs[0]));
      asm volatile("" : "+v"(acc1[n]));
      SBAR();
    };
    iter(fB, fA, I1{}, I1{}); z1act(0); BARRIER();
    iter(fA, fB, I1{}, I1{}); z1act(1); BARRIER();
    iter(fB, fA, I1{}, I1{}); z1act(2); BARRIER();
    iter(fA, fB, I1{}, I1{}); BARRIER();
    u32x4 xv0, xv1;
    unsigned go0, go1, la0, la1;
    {
      int i0 = tid, i1 = tid < 128 ? tid + 512 : tid;
      go0 = pinv((rg3 * 64 + i0 / 10) * D_ + cb3 * 80 + (i0 % 10) * 8);
      go1 = pinv((rg3 * 64 + i1 / 10) * D_ + cb3 * 80 + (i1 % 10) * 8);
      la0 = pinv(lds0 + (i0 / 10) * (80 * 4 + 16) + (i0 % 10) * 32);
      la1 = pinv(lds0 + (i1 / 10) * (80 * 4 + 16) + (i1 % 10) * 32);
      xv0 = *(const u32x4*)(p.x + go0);
      xv1 = *(const u32x4*)(p.x + go1);
    }
    iter(fB, fA, I1{}, I1{}); BARRIER();
#pragma unroll 1
    for (int t = 24; t < 34; t += 2) {
      iter(fA, fB, I1{}, I1{}); BARRIER();
      iter(fB, fA, I1{}, I1{}); BARRIER();
    }
    iter(fA, fB, I1{}, I1{}); BARRIER();
    iter(fB, fA, I1{}, I1{}); BARRIER();
#pragma unroll 1
    for (int t = 36; t < 40; t += 2) {
      iter(fA, fB, I1{}, I1{}); BARRIER();
      iter(fB, fA, I1{}, I1{}); BARRIER();
    }
    iter(fA, fB, I1{}, I1{}); BARRIER();
    iter(fB, fA, I1{}, I2{});
    STAMP(1, 42);
    constexpr int OST = 96 * 2 + 16;
    {
      unsigned dst = scr + urow * OST + (wc * 48 + g * 4) * 2;
#pragma unroll
      for (int n = 0; n < 3; ++n) {
        float v[4];
#pragma unroll
        for (int r = 0; r < 4; ++r) v[r] = acc1[n][r] * (acc2[n][r] + bias);
        *(LAS u32x2*)(uintptr_t)(dst + n * 32) = u32x2{pack_bf16(v[0], v[1]), pack_bf16(v[2], v[3])};
      }
    }
    SBAR();
    WAIT_LGKM0();
    BARRIER();
    iter(fA, fB, I2{}, I2{}); BARRIER();
    STAMP(1, 43);
    iter(fB, fA, I2{}, I2{}); BARRIER();
    iter(fA, fB, I2{}, I2{}); BARRIER();
    iter(fB, fA, I2{}, I2{}); BARRIER();
    STAMP(1, 46);
#pragma unroll 1
    for (int t = 46; t < 50; t += 2) {
      iter(fA, fB, I2{}, I2{}); BARRIER();
      iter(fB, fA, I2{}, I2{}); BARRIER();
    }
    iter(fA, fB, I2{}, I2{}); BARRIER();
    STAMP(1, 51);
    iter(fB, fA, I2{}, I3{});
    BARRIER();
    STAMP(1, 52);
    BARRIER();
    {
      unsigned tmp = 0;
#pragma unroll
      for (int i = 0; i < 8; ++i) ldf(fA, 2, i, tmp);
      adv();
    }
    BARRIER();
    STAMP(1, 53);
#pragma unroll 1
    for (int t = T_B; t < T_END; t += 2) {
      iter(fA, fB, I2{}, I2{}); BARRIER();
      iter(fB, fA, I2{}, I2{});
      if (t == T_END - 2) WAIT_LGKM0();
      BARRIER();
    }
    STAMP(1, 100);
    constexpr int OS3 = 80 * 4 + 16;
    {
      unsigned dst = lds0 + kh * (64 * OS3) + (wm * 16 + fr) * OS3 + g * 16;
#pragma unroll
      for (int n = 0; n < 5; ++n) *(LAS f32x4*)(uintptr_t)(dst + n * 64) = acc3[n];
    }
    WAIT_LGKM0();
    BARRIER();
#pragma unroll
    for (int it = 0; it < 2; ++it) {
      if (it == 0 || tid < 128) {
        u32x4 xv = it ? xv1 : xv0;
        unsigned la = it ? la1 : la0;
        f32x4 a = *(LAS f32x4*)(uintptr_t)(la) + *(LAS f32x4*)(uintptr_t)(la + 64 * OS3);
        f32x4 c4 = *(LAS f32x4*)(uintptr_t)(la + 16) + *(LAS f32x4*)(uintptr_t)(la + 64 * OS3 + 16);
        u32x4 o = {pack_bf16(bf_lo(xv[0]) + a[0], bf_hi(xv[0]) + a[1]), pack_bf16(bf_lo(xv[1]) + a[2], bf_hi(xv[1]) + a[3]),
                   pack_bf16(bf_lo(xv[2]) + c4[0], bf_hi(xv[2]) + c4[1]), pack_bf16(bf_lo(xv[3]) + c4[2], bf_hi(xv[3]) + c4[3])};
        *(u32x4*)(p.out + (it ? go1 : go0)) = o;
      }
    }
    STAMP(1, 201);
  }
}

extern "C" void kernel_launch(void* const* d_in, const int* in_sizes, int n_in, void* d_out, int out_size, void* d_ws,
                              size_t ws_size, hipStream_t stream) {
  Params p;
  p.x = (const u16*)d_in[0];
  p.wqkv = (const u16*)d_in[1];
  p.wo = (const u16*)d_in[2];
  p.w_in = (const u16*)d_in[4];
  p.w_sp = (const u16*)d_in[5];
  p.b_sp = (const u16*)d_in[6];
  p.w_out = (const u16*)d_in[7];
  p.out = (u16*)d_out;
  p.ws = (char*)d_ws;
  (void)hipFuncSetAttribute((const void*)stage1_kernel, hipFuncAttributeMaxDynamicSharedMemorySize, S1_LDS);
  (void)hipFuncSetAttribute((const void*)stage2_kernel, hipFuncAttributeMaxDynamicSharedMemorySize, K2_LDS);
  stage1_kernel<<<256, S1_THREADS, S1_LDS, stream>>>(p);
  stage2_kernel<<<256, K2_THREADS, K2_LDS, stream>>>(p);
}

#if defined(__HIP_DEVICE_COMPILE__) && __HIP_DEVICE_COMPILE__
#pragma clang attribute pop
#endif
```

```cpp
#if defined(__HIP_DEVICE_COMPILE__) && __HIP_DEVICE_COMPILE__
#pragma clang attribute push(__attribute__((target("no-packed-fp32-ops"))), apply_to = function)
#endif
#include <hip/hip_runtime.h>
#include <hip/hip_bf16.h>
#include <stdint.h>
#include <type_traits>

typedef short bf16x8 __attribute__((ext_vector_type(8)));
typedef short s16x4 __attribute__((ext_vector_type(4)));
typedef float f32x4 __attribute__((ext_vector_type(4)));
typedef float f32x2 __attribute__((ext_vector_type(2)));
typedef unsigned u32x4 __attribute__((ext_vector_type(4)));
typedef unsigned u32x2 __attribute__((ext_vector_type(2)));
typedef __bf16 bf16x2_t __attribute__((ext_vector_type(2)));
typedef unsigned short u16;

#define TIDX ((int)__builtin_amdgcn_workitem_id_x())
#define BIDX ((int)__builtin_amdgcn_workgroup_id_x())
#define LAS __attribute__((address_space(3)))
#define DI __device__ __forceinline__

constexpr int S_ = 2048, D_ = 640, H_ = 32, DH_ = 20, F_ = 768, KA_ = D_ + F_;

constexpr size_t WS_Y2 = 0;
constexpr size_t WS_Q = WS_Y2 + (size_t)S_ * F_ * 2;
constexpr size_t WS_UA = WS_Q + (size_t)S_ * D_ * 2;
constexpr size_t WS_B3 = WS_UA + (size_t)S_ * KA_ * 2;
constexpr size_t WS_KVP = WS_B3 + (size_t)KA_ * D_ * 2;
constexpr size_t WS_XST = WS_KVP + (size_t)16 * 32 * 420 * 4;
constexpr size_t WS_Z2N = WS_XST + (size_t)S_ * 2 * 4;
constexpr size_t WS_END = WS_Z2N + (size_t)S_ * F_ * 2;
constexpr size_t WS_FLAGS = WS_END;
constexpr size_t WS_KVR = WS_FLAGS + 64 * 32 * 4;

DI unsigned pack_bf16(float a, float b) {
  f32x2 v = {a, b};
  bf16x2_t r = __builtin_convertvector(v, bf16x2_t);
  return __builtin_bit_cast(unsigned, r);
}
DI float bf_lo(unsigned w) { return __builtin_bit_cast(float, w << 16); }
DI float bf_hi(unsigned w) { return __builtin_bit_cast(float, w & 0xffff0000u); }
DI float phi_f(float t) {
  return (fmaxf(t, 0.f) + 1e-6f) + __builtin_amdgcn_exp2f(fminf(t, 0.f) * 1.4426950408889634f);
}
DI float gelu_f(float x) {
  constexpr float c1 = -2.f * 0.7978845608028654f * 1.4426950408889634f;
  constexpr float c3 = c1 * 0.044715f;
  float a = x * __builtin_fmaf(x * x, c3, c1);
  return x * __builtin_amdgcn_rcpf(1.f + __builtin_amdgcn_exp2f(a));
}
DI float wave_sum(float v) {
  v += __builtin_bit_cast(float, __builtin_amdgcn_update_dpp(0, __builtin_bit_cast(int, v), 0xB1, 0xf, 0xf, false));
  v += __builtin_bit_cast(float, __builtin_amdgcn_update_dpp(0, __builtin_bit_cast(int, v), 0x4E, 0xf, 0xf, false));
  v += __builtin_bit_cast(float, __builtin_amdgcn_update_dpp(0, __builtin_bit_cast(int, v), 0x141, 0xf, 0xf, false));
  v += __builtin_bit_cast(float, __builtin_amdgcn_update_dpp(0, __builtin_bit_cast(int, v), 0x140, 0xf, 0xf, false));
  int iv = __builtin_bit_cast(int, v);
  float r0 = __builtin_bit_cast(float, __builtin_amdgcn_readlane(iv, 0)), r1 = __builtin_bit_cast(float, __builtin_amdgcn_readlane(iv, 16));
  float r2 = __builtin_bit_cast(float, __builtin_amdgcn_readlane(iv, 32)), r3 = __builtin_bit_cast(float, __builtin_amdgcn_readlane(iv, 48));
  return (r0 + r1) + (r2 + r3);
}
DI f32x4 mfma16(bf16x8 a, bf16x8 b, f32x4 c) { return __builtin_amdgcn_mfma_f32_16x16x32_bf16(a, b, c, 0, 0, 0); }
DI bf16x8 lds_b128(unsigned addr) { return *(LAS bf16x8*)(uintptr_t)addr; }
DI s16x4 lds_tr1(unsigned a) { return __builtin_amdgcn_ds_read_tr16_b64_v4i16((LAS s16x4*)(uintptr_t)a); }
DI bf16x8 cat8(s16x4 lo, s16x4 hi) { return __builtin_shufflevector(lo, hi, 0, 1, 2, 3, 4, 5, 6, 7); }
DI void glds16(const void* sbase, unsigned voff, unsigned lds_addr) {
  unsigned sv;
  asm volatile("s_mov_b32 %0, m0\n\ts_mov_b32 m0, %3\n\ts_nop 0\n\tglobal_load_lds_dwordx4 %1, %2\n\ts_mov_b32 m0, %0"
               : "=&s"(sv) : "v"(voff), "s"(sbase), "s"(lds_addr));
}
DI void touch_line(unsigned& sink, const void* sbase, unsigned voff) {
  asm volatile("global_load_dword %0, %1, %2" : "=v"(sink) : "v"(voff), "s"(sbase));
}
#define WAIT_VM(n) asm volatile("s_waitcnt vmcnt(%0)" ::"i"(n) : "memory")
#define WAIT_LGKM0() asm volatile("s_waitcnt lgkmcnt(0)" ::: "memory")
#define BARRIER() do { asm volatile("" ::: "memory"); __builtin_amdgcn_s_barrier(); asm volatile("" ::: "memory"); } while (0)
#define SBAR() __builtin_amdgcn_sched_barrier(0)
DI unsigned pinv(unsigned a) { asm volatile("" : "+v"(a)); return a; }

struct Params {
  const u16* x;
  const u16* wqkv;
  const u16* wo;
  const u16* w_in;
  const u16* w_sp;
  const u16* b_sp;
  const u16* w_out;
  u16* out;
  char* ws;
};

template <bool XQ>
DI int bswz(int k, int c) { return c ^ (((k >> 3) & 1) << 1) ^ (XQ ? (((k >> 1) & 1) << 2) : 0); }

constexpr int S1_STAGE = 16384 + 24576;
constexpr int S1_NBUF = 4;
constexpr int S1_LDS = S1_NBUF * S1_STAGE;
constexpr int S1_THREADS = 768;

template <int TYPE>
DI void stage1_body(const Params& p, int rg, int ct, unsigned lds0, int xcd, int jb) {
  constexpr int BN = TYPE == 2 ? 192 : 160;
  constexpr int CPR = BN / 8;
  constexpr int NF = BN / 32;
  constexpr int BI = BN / 32;
  constexpr int G = 4 + BI;
  constexpr int NT = D_ / 64;
  constexpr bool XQ = TYPE == 2;
  const int tid = TIDX, lane = tid & 63;
  const int wid = __builtin_amdgcn_readfirstlane(tid >> 6);
  const int fr = lane & 15, g = lane >> 4;
  const int wr = (wid >> 1) & 3, wc = wid & 1;

  LAS float* st_rs = (LAS float*)(uintptr_t)lds0;
  LAS float* st_sq = st_rs + 128;
  LAS float* st_cs = (LAS float*)(uintptr_t)(lds0 + 1024);
  const unsigned otile = lds0 + 4096;
  constexpr int OST = BN * 2 + 16;

  unsigned gofs[4] = {0, 0, 0, 0};
  if (TYPE == 0 && wid < 8) {
#pragma unroll
    for (int it = 0; it < 4; ++it) {
      int i = tid + it * 512;
      if (i >= 1680) i = 0;
      int hl = i / 420, w = i - hl * 420;
      int hi = hl & 1, ci, cj, q;
      if (w < 400) {
        int d = w / 20, e = w - d * 20;
        ci = hi * 20 + d; cj = 40 + hi * 20 + e;
        int pi = ci >> 4, pj = cj >> 4;
        q = pi == 0 ? pj - 2 : (pi == 1 ? pj : pj + 2);
      } else {
        ci = hi * 20 + (w - 400); cj = 0;
        q = 7 + (ci >> 4);
      }
      gofs[it] = pinv((hl >> 1) * 10240 + q * 1024 + ((((ci & 15) >> 2) * 16 + (cj & 15)) * 4 + (ci & 3)) * 4);
    }
  }

  if (wid >= 8) {
    const int lw = wid - 8;
    const u16* Bm = TYPE == 2 ? p.w_in : p.wqkv;
    constexpr int ldb = TYPE == 2 ? 2 * F_ : 3 * D_;
    unsigned aoff, boff[BI];
    {
      int row = lw * 8 + (lane >> 3), c = (lane & 7) ^ ((row >> 1) & 7);
      aoff = ((rg * 128 + row) * D_ + c * 8) * 2;
    }
#pragma unroll
    for (int i = 0; i < BI; ++i) {
      int slot = (i * 4 + lw) * 64 + lane;
      int k = slot / CPR, c = bswz<XQ>(k, slot - k * CPR);
      int col;
      if (TYPE == 0) col = c < 10 ? (D_ + ct * 80 + c * 8) : (2 * D_ + ct * 80 + (c - 10) * 8);
      else if (TYPE == 1) col = ct * 160 + c * 8;
      else col = F_ + ct * 192 + c * 8;
      boff[i] = (k * ldb + col) * 2;
    }
    auto issue = [&](int t) {
      unsigned base = lds0 + (t & 3) * S1_STAGE;
#pragma unroll
      for (int i = 0; i < 4; ++i) glds16(p.x + t * 64 + i * 32 * D_, aoff, base + (i * 4 + lw) * 1024);
#pragma unroll
      for (int i = 0; i < BI; ++i) glds16(Bm + (size_t)t * 64 * ldb, boff[i], base + 16384 + (i * 4 + lw) * 1024);
    };
    issue(0);
    issue(1);
    unsigned sk[5];
    WAIT_VM(G);
    BARRIER();
    issue(2);
    WAIT_VM(G);
    BARRIER();
#pragma unroll 1
    for (int t = 0; t < NT - 4; ++t) {
      issue(t + 3);
      WAIT_VM(G);
      BARRIER();
    }
    issue(NT - 1);
    WAIT_VM(G);
    BARRIER();
    WAIT_VM(0);
    BARRIER();
    if (TYPE != 0) {
      const int lt = lw * 64 + lane;
      const int i16 = (TYPE - 1) * 8 + (jb & 7);
      const int i128 = xcd * 16 + i16;
      touch_line(sk[0], p.w_sp, ((((xcd & 3) * 2 + (xcd >> 2)) * 16 + i16) * 512 + lt) * 128);
      touch_line(sk[1], p.w_sp, ((((xcd & 3) * 2 + (xcd >> 2)) * 16 + i16) * 512 + 256 + lt) * 128);
      int i = lt < 60 ? lt : 0;
      touch_line(sk[2], p.w_in, (((xcd & 3) * 160 + i16 * 10 + i / 6) * (2 * F_) + (xcd >> 2) * 384) * 2 + (i % 6) * 128);
      touch_line(sk[3], p.w_out, (i128 * 60 + (lt < 60 ? lt : 0)) * 128);
      touch_line(sk[4], p.wo, (i128 * 50 + (lt < 50 ? lt : 0)) * 128);
    }
    BARRIER();
    BARRIER();
    BARRIER();
    BARRIER();
    if (TYPE != 0) asm volatile("s_waitcnt vmcnt(0)" : "+v"(sk[0]), "+v"(sk[1]), "+v"(sk[2]), "+v"(sk[3]), "+v"(sk[4]) :: "memory");
    return;
  } else {
    unsigned abase[2], baddr[NF];
    {
      int x7 = (fr >> 1) & 7;
#pragma unroll
      for (int ks = 0; ks < 2; ++ks) abase[ks] = lds0 + (wr * 32 + wc * 16 + fr) * 128 + (((ks * 4 + g) ^ x7) << 4);
      int q = fr >> 2, pp = fr & 3;
#pragma unroll
      for (int n = 0; n < NF; ++n) {
        int c;
        if (TYPE == 0) {
          int w = 2 * n + (pp >> 1);
          c = w < 5 ? wc * 5 + w : 10 + wc * 5 + (w - 5);
        } else {
          c = 2 * (wc * NF + n) + (pp >> 1);
        }
        int k = 8 * g + q;
        baddr[n] = lds0 + 16384 + (k * CPR + bswz<XQ>(k, c)) * 16 + (pp & 1) * 8;
      }
    }
    const int am1 = wc ? -2048 : 2048;
    f32x4 acc[2][NF], cs[NF], rs, sq;
#pragma unroll
    for (int m = 0; m < 2; ++m)
#pragma unroll
      for (int n = 0; n < NF; ++n) acc[m][n] = f32x4{0, 0, 0, 0};
#pragma unroll
    for (int n = 0; n < NF; ++n) cs[n] = f32x4{0, 0, 0, 0};
    rs = f32x4{0, 0, 0, 0};
    sq = f32x4{0, 0, 0, 0};
    const short one = 0x3f80;
    bf16x8 ones = {one, one, one, one, one, one, one, one};
    asm volatile("" : "+v"(ones));

    bf16x8 af0[2], af1[2];
    s16x4 bl0[NF], bh0[NF], bl1[NF], bh1[NF];
    auto phase = [&](bf16x8 (&afc)[2], s16x4 (&blc)[NF], s16x4 (&bhc)[NF], bf16x8 (&afn)[2], s16x4 (&bln)[NF], s16x4 (&bhn)[NF],
                     unsigned sbn, int ksn, bool docs) {
      const unsigned a0 = pinv(abase[ksn] + sbn);
      unsigned ba = 0;
#pragma unroll
      for (int i = 0; i < 2 * NF + 2; ++i) {
        if (i < 2 * NF) {
          int m = i / NF, n = i % NF;
          bf16x8 bq = cat8(blc[n], bhc[n]);
          acc[m][n] = TYPE == 0 ? mfma16(afc[m], bq, acc[m][n]) : mfma16(bq, afc[m], acc[m][n]);
        } else if (i == 2 * NF) rs = mfma16(afc[0], ones, rs);
        else sq = mfma16(afc[0], afc[0], sq);
        if (i == 0) afn[0] = lds_b128(a0);
        else if (i <= 2 * NF) {
          int r = i - 1;
          if (r & 1) bhn[r >> 1] = lds_tr1(ba + (ksn * 32 + 4) * CPR * 16);
          else { ba = pinv(baddr[r >> 1] + sbn); bln[r >> 1] = lds_tr1(ba + ksn * 32 * CPR * 16); }
        } else afn[1] = lds_b128(a0 + am1);
        SBAR();
      }
      if (docs) {
#pragma unroll
        for (int n = 0; n < NF; ++n) cs[n] = mfma16(ones, cat8(blc[n], bhc[n]), cs[n]);
      }
      SBAR();
    };
    BARRIER();
    af0[0] = lds_b128(abase[0]);
    af0[1] = lds_b128(abase[0] + am1);
#pragma unroll
    for (int n = 0; n < NF; ++n) { bl0[n] = lds_tr1(baddr[n]); bh0[n] = lds_tr1(baddr[n] + 4 * CPR * 16); }
    phase(af0, bl0, bh0, af1, bl1, bh1, 0, 1, wr == 0);
    BARRIER();
    phase(af1, bl1, bh1, af0, bl0, bh0, S1_STAGE, 0, wr == 1);
    BARRIER();
#pragma unroll 1
    for (int t = 1; t < NT; ++t) {
      phase(af0, bl0, bh0, af1, bl1, bh1, (t & 3) * S1_STAGE, 1, wr == ((2 * t) & 3));
      phase(af1, bl1, bh1, af0, bl0, bh0, ((t + 1) & 3) * S1_STAGE, 0, wr == ((2 * t + 1) & 3));
      if (t == NT - 1) WAIT_LGKM0();
      BARRIER();
    }
    {
      int r0 = wr * 32 + wc * 16;
      if (fr == 0) *(LAS f32x4*)(st_rs + r0 + g * 4) = rs;
      if ((fr >> 2) == g) {
        int r = fr & 3;
        st_sq[r0 + fr] = r == 0 ? sq[0] : r == 1 ? sq[1] : r == 2 ? sq[2] : sq[3];
      }
      if (g == 0) {
#pragma unroll
        for (int n = 0; n < NF; ++n) st_cs[wr * 192 + wc * NF * 16 + n * 16 + fr] = cs[n][0];
      }
    }
    WAIT_LGKM0();
    BARRIER();
    if (TYPE == 0) {
      float csv[NF];
#pragma unroll
      for (int n = 0; n < NF; ++n) {
        int c0 = wc * NF * 16 + n * 16 + fr;
        csv[n] = (st_cs[c0] + st_cs[192 + c0]) + (st_cs[384 + c0] + st_cs[576 + c0]);
      }
      bf16x8 xf[NF];
#pragma unroll
      for (int m = 0; m < 2; ++m) {
        int r0 = wr * 32 + (m ^ wc) * 16 + g * 4;
        f32x4 mean = *(LAS f32x4*)(st_rs + r0) * (1.f / D_);
        f32x4 ex2 = *(LAS f32x4*)(st_sq + r0) * (1.f / D_);
        f32x4 rstd, nm;
#pragma unroll
        for (int r = 0; r < 4; ++r) {
          rstd[r] = __builtin_amdgcn_rsqf(fmaxf(ex2[r] - mean[r] * mean[r], 0.f) + 1e-5f);
          nm[r] = -mean[r] * rstd[r];
        }
#pragma unroll
        for (int n = 0; n < NF; ++n) {
          f32x4 y = acc[m][n] * rstd + nm * csv[n];
          bool isk = (n * 16 + fr) < 40;
          float v[4];
#pragma unroll
          for (int r = 0; r < 4; ++r) v[r] = n < 2 ? phi_f(y[r]) : (n == 2 ? (isk ? phi_f(y[r]) : y[r]) : y[r]);
          u32x4 t4 = __builtin_bit_cast(u32x4, xf[n]);
          t4[m * 2] = pack_bf16(v[0], v[1]);
          t4[m * 2 + 1] = pack_bf16(v[2], v[3]);
          xf[n] = __builtin_bit_cast(bf16x8, t4);
        }
      }
      SBAR();
      constexpr int PI[7] = {0, 0, 1, 1, 1, 2, 2};
      constexpr int PJ[7] = {2, 3, 2, 3, 4, 3, 4};
      f32x4 dd[10];
#pragma unroll
      for (int q = 0; q < 7; ++q) dd[q] = mfma16(xf[PI[q]], xf[PJ[q]], f32x4{0, 0, 0, 0});
#pragma unroll
      for (int i = 0; i < 3; ++i) dd[7 + i] = mfma16(xf[i], ones, f32x4{0, 0, 0, 0});
      SBAR();
#pragma unroll
      for (int q = 0; q < 10; ++q) *(LAS f32x4*)(uintptr_t)(otile + wid * 10240 + q * 1024 + lane * 16) = dd[q];
    } else {
      f32x4 csv[NF];
#pragma unroll
      for (int n = 0; n < NF; ++n) {
        int c0 = wc * NF * 16 + n * 16 + g * 4;
        csv[n] = (*(LAS f32x4*)(st_cs + c0) + *(LAS f32x4*)(st_cs + 192 + c0)) + (*(LAS f32x4*)(st_cs + 384 + c0) + *(LAS f32x4*)(st_cs + 576 + c0));
      }
#pragma unroll
      for (int m = 0; m < 2; ++m) {
        int rl = wr * 32 + (m ^ wc) * 16 + fr;
        float mean = st_rs[rl] * (1.f / D_);
        float ex2 = st_sq[rl] * (1.f / D_);
        float rstd = __builtin_amdgcn_rsqf(fmaxf(ex2 - mean * mean, 0.f) + 1e-5f);
        float nm = -mean * rstd;
        if (TYPE == 2 && ct == 0 && m == 0 && g == 0) *(f32x2*)((float*)(p.ws + WS_XST) + (size_t)(rg * 128 + rl) * 2) = f32x2{nm, rstd};
        unsigned dst = otile + rl * OST + (wc * NF * 16 + g * 4) * 2;
#pragma unroll
        for (int n = 0; n < NF; ++n) {
          f32x4 y = acc[m][n] * rstd + csv[n] * nm;
          *(LAS u32x2*)(uintptr_t)(dst + n * 32) = u32x2{pack_bf16(y[0], y[1]), pack_bf16(y[2], y[3])};
        }
      }
    }
  }
  WAIT_LGKM0();
  BARRIER();
  if (TYPE == 0) {
    float* kvp = (float*)(p.ws + WS_KVP) + ((size_t)rg * 32 + ct * 4) * 420;
#pragma unroll
    for (int it = 0; it < 4; ++it) {
      int i = tid + it * 512;
      if (i < 1680) {
        unsigned a = otile + gofs[it];
        float v = (*(LAS float*)(uintptr_t)(a) + *(LAS float*)(uintptr_t)(a + 2 * 10240)) +
                  (*(LAS float*)(uintptr_t)(a + 4 * 10240) + *(LAS float*)(uintptr_t)(a + 6 * 10240));
        kvp[i] = v;
      }
    }
  } else {
    u16* dstm = TYPE == 1 ? (u16*)(p.ws + WS_Q) + (size_t)rg * 128 * D_ + ct * 160
                          : (u16*)(p.ws + WS_Y2) + (size_t)rg * 128 * F_ + ct * 192;
    constexpr int ldo = TYPE == 1 ? D_ : F_;
    for (int idx = tid; idx < 128 * CPR; idx += 512) {
      int row = idx / CPR, c = idx - row * CPR;
      u32x4 v = *(LAS u32x4*)(uintptr_t)(otile + row * OST + c * 16);
      *(u32x4*)(dstm + (size_t)row * ldo + c * 8) = v;
    }
  }
}

__global__ __launch_bounds__(S1_THREADS, 3) void stage1_kernel(Params p) {
  extern __shared__ __attribute__((aligned(16))) char shm[];
  unsigned lds0 = (unsigned)(uintptr_t)(LAS char*)shm;
  int b = BIDX, xcd = b & 7, j = b >> 3;
  int rg = (xcd & 1) * 8 + (j & 7);
  int cq = xcd >> 1, ci = j >> 3;
  if (b == 0) {
    for (int i = TIDX; i < 64 * 32; i += S1_THREADS) ((unsigned*)(p.ws + WS_FLAGS))[i] = 0u;
  }
  if (ci < 2) stage1_body<0>(p, rg, cq * 2 + ci, lds0, xcd, j);
  else if (ci == 2) stage1_body<1>(p, rg, cq, lds0, xcd, j);
  else stage1_body<2>(p, rg, cq, lds0, xcd, j);
}

constexpr int K2_SLOT = 8192 + 12288;
constexpr int K2_NS = 7;
constexpr int K2_SCR = K2_NS * K2_SLOT;
constexpr int K2_LDS = K2_SCR + 20480;
constexpr int K2_MID = (K2_NS - 1) * K2_SLOT;
constexpr int K2_THREADS = 1024;
constexpr int T_G = 10, T_A = 42, T_B = 52, T_END = 64;
constexpr unsigned SPIN_CAP = 1u << 22;

typedef __attribute__((address_space(1))) unsigned gu32;
DI void* uni_ptr(const void* q) {
  unsigned long long a = (unsigned long long)q;
  unsigned lo = __builtin_amdgcn_readfirstlane((unsigned)a), hi = __builtin_amdgcn_readfirstlane((unsigned)(a >> 32));
  return (void*)(((unsigned long long)hi << 32) | lo);
}
DI void store16_sc1(void* sbase, unsigned voff, u32x4 v) {
  sbase = uni_ptr(sbase);
  asm volatile("s_nop 4\n\tglobal_store_dwordx4 %0, %1, %2 sc1\n\ts_nop 1" ::"v"(voff), "v"(v), "s"(sbase) : "memory");
}
DI void store8_sc1(void* sbase, unsigned voff, u32x2 v) {
  asm volatile("global_store_dwordx2 %0, %1, %2 sc1\n\ts_nop 1" ::"v"(voff), "v"(v), "s"(sbase) : "memory");
}
DI void glds16_sc1(const void* sbase, unsigned voff, unsigned lds_addr) {
  unsigned sv;
  asm volatile("s_mov_b32 %0, m0\n\ts_mov_b32 m0, %3\n\ts_nop 0\n\tglobal_load_lds_dwordx4 %1, %2 sc1\n\ts_mov_b32 m0, %0"
               : "=&s"(sv) : "v"(voff), "s"(sbase), "s"(lds_addr));
}
DI void wait_flags(unsigned* flags, int nw, unsigned target, int lane) {
  gu32* fp = (gu32*)(flags + (lane < nw ? lane : 0) * 32);
  for (unsigned spins = 0; spins < SPIN_CAP; ++spins) {
    unsigned v = __hip_atomic_load(fp, __ATOMIC_RELAXED, __HIP_MEMORY_SCOPE_AGENT);
    if (__all(v >= target)) break;
    __builtin_amdgcn_s_sleep(1);
  }
  __builtin_amdgcn_fence(__ATOMIC_ACQUIRE, "wavefront");
}

__global__ __launch_bounds__(K2_THREADS, 4) void stage2_kernel(Params p) {
  extern __shared__ __attribute__((aligned(16))) char shm[];
  const unsigned lds0 = (unsigned)(uintptr_t)(LAS char*)shm;
  const int b = BIDX, xcd = b & 7, j = b >> 3;
  const int rg = (xcd & 3) * 8 + (j & 7);
  const int cb = (xcd >> 2) * 4 + (j >> 3);
  const int rg3 = xcd * 4 + (j & 3), cb3 = j >> 2;
  const int tid = TIDX, lane = tid & 63;
  const int wid = __builtin_amdgcn_readfirstlane(tid >> 6);
  const int fr = lane & 15, g = lane >> 4;
  unsigned* flags = (unsigned*)(p.ws + WS_FLAGS);
  constexpr int CPR = 12;

  if (wid >= 12) {
    const int st = tid - 768, sw = wid - 12;
    const int mcb = b & 7;
    const unsigned scr = lds0 + K2_SCR;
    LAS float* zt = (LAS float*)(uintptr_t)(scr + 18432);
#define SB(n) do { _Pragma("unroll 1") for (int i_ = 0; i_ < (n); ++i_) BARRIER(); } while (0)
#define FLAG_ADD_S(off) do { if (st == 0) __hip_atomic_fetch_add((gu32*)(flags + (off)), 1u, __ATOMIC_RELAXED, __HIP_MEMORY_SCOPE_AGENT); } while (0)
    u32x4 w0, w1;
    u32x2 qw[5];
    {
      const u16* wsrc = b < 128 ? p.wo + (size_t)(b >> 3) * 40 * D_ : p.w_out + (size_t)((b - 128) >> 3) * 48 * D_;
      const int nch = b < 128 ? 400 : 480;
      int i1 = st + 256 < nch ? st + 256 : 0;
      w0 = *(const u32x4*)(wsrc + (st / 10) * D_ + mcb * 80 + (st % 10) * 8);
      w1 = *(const u32x4*)(wsrc + (i1 / 10) * D_ + mcb * 80 + (i1 % 10) * 8);
      const u16* qsrc = (const u16*)(p.ws + WS_Q) + (size_t)((b >> 1) * 16 + (st >> 4)) * D_ + (b & 1) * 320 + (st & 15) * 20;
#pragma unroll
      for (int q = 0; q < 5; ++q) qw[q] = *(const u32x2*)(qsrc + q * 4);
    }
    SB(3);
    float pq[20];
#pragma unroll
    for (int q = 0; q < 3; ++q) {
      pq[q * 4] = phi_f(bf_lo(qw[q][0])); pq[q * 4 + 1] = phi_f(bf_hi(qw[q][0]));
      pq[q * 4 + 2] = phi_f(bf_lo(qw[q][1])); pq[q * 4 + 3] = phi_f(bf_hi(qw[q][1]));
    }
    SBAR();
    SB(1);
#pragma unroll
    for (int q = 3; q < 5; ++q) {
      pq[q * 4] = phi_f(bf_lo(qw[q][0])); pq[q * 4 + 1] = phi_f(bf_hi(qw[q][0]));
      pq[q * 4 + 2] = phi_f(bf_lo(qw[q][1])); pq[q * 4 + 3] = phi_f(bf_hi(qw[q][1]));
    }
    *(LAS u32x4*)(uintptr_t)(scr + 8192 + st * 16) = w0;
    if (st + 256 < (b < 128 ? 400 : 480)) *(LAS u32x4*)(uintptr_t)(scr + 8192 + (st + 256) * 16) = w1;
    if (b < 128) {
      *(LAS u32x4*)(uintptr_t)(scr + st * 16) = u32x4{0, 0, 0, 0};
      *(LAS u32x4*)(uintptr_t)(scr + 4096 + st * 16) = u32x4{0, 0, 0, 0};
      if (st < 240) *(LAS u32x4*)(uintptr_t)(scr + 8192 + 6400 + st * 16) = u32x4{0, 0, 0, 0};
    }
    WAIT_LGKM0();
    SB(3);
    if (sw == 0) wait_flags(flags, 16, 16u, lane);
    SB(1);
    u32x4 tb0, tb1;
    {
      unsigned o0 = (((b < 128 ? b >> 3 : 0) * 2 + (st < 200 ? st / 100 : 0)) * 420 + (st % 100) * 4) * 4;
      int i4 = (st < 80 ? st : 0) * 4;
      unsigned o1 = (((b & 1) * 16 + i4 / 20) * 420 + 400 + i4 % 20) * 4;
      asm volatile("global_load_dwordx4 %0, %1, %2 sc1" : "=v"(tb0) : "v"(o0), "s"(p.ws + WS_KVR));
      asm volatile("global_load_dwordx4 %0, %1, %2 sc1" : "=v"(tb1) : "v"(o1), "s"(p.ws + WS_KVR));
    }
    const int q4 = fr >> 2, pp = fr & 3;
    if (b >= 128) {
#pragma unroll
      for (int it = 0; it < 2; ++it) {
        int blk = (it * 4 + sw) * 4 + g;
        int bb = blk < 30 ? blk : 0;
        int kc = bb / 5, nf = bb - kc * 5;
        unsigned wa = scr + 8192 + (kc * 8 + q4) * 160 + nf * 32 + pp * 8;
        bf16x8 v = cat8(lds_tr1(wa), lds_tr1(wa + 4 * 160));
        if (blk < 30) store16_sc1(p.ws + WS_B3, ((mcb * 176 + 80 + ((b - 128) >> 3) * 6 + kc) * 80 + nf * 16 + fr) * 16, __builtin_bit_cast(u32x4, v));
      }
    }
    SB(5);
    asm volatile("s_waitcnt vmcnt(0)" : "+v"(tb0), "+v"(tb1) :: "memory");
    if (st < 80) *(LAS u32x4*)(zt + st * 4) = tb1;
    if (b < 128 && st < 200) {
      int hh = st / 100, w = (st - hh * 100) * 4, dl = w / 20, el = w - dl * 20;
      int d = hh * 20 + dl, e = hh * 20 + el;
      int row = (d >> 5) * 32 + ((d >> 2) & 1) * 16 + ((d >> 3) & 3) * 4 + (d & 3);
      f32x4 kf = __builtin_bit_cast(f32x4, tb0);
      unsigned a = scr + row * 128 + ((((e >> 3) ^ ((row >> 1) & 7)) << 4) | ((e & 4) << 1));
      *(LAS u32x2*)(uintptr_t)a = u32x2{pack_bf16(kf[0], kf[1]), pack_bf16(kf[2], kf[3])};
    }
    WAIT_LGKM0();
    SB(1);
    {
      float den = 0.f;
#pragma unroll
      for (int q = 0; q < 5; ++q) {
        f32x4 zz = *(LAS f32x4*)(zt + (st & 15) * 20 + q * 4);
        den += pq[q * 4] * zz[0] + pq[q * 4 + 1] * zz[1] + pq[q * 4 + 2] * zz[2] + pq[q * 4 + 3] * zz[3];
      }
      float inv = 1.f / (den + 1e-6f);
#pragma unroll
      for (int q = 0; q < 5; ++q)
        qw[q] = u32x2{pack_bf16(pq[q * 4] * inv, pq[q * 4 + 1] * inv), pack_bf16(pq[q * 4 + 2] * inv, pq[q * 4 + 3] * inv)};
    }
    SBAR();
    SB(1);
#pragma unroll
    for (int it = 0; it < 3; ++it) {
      if (b < 128) {
        int unit = sw + it * 4;
        int u = unit < 10 ? unit : 0;
        int db = u / 5, nf = u - db * 5;
        f32x4 d0 = {0, 0, 0, 0}, d1 = {0, 0, 0, 0};
#pragma unroll
        for (int ks = 0; ks < 2; ++ks) {
          unsigned wa = scr + 8192 + (ks * 32 + g * 8 + q4) * 160 + nf * 32 + pp * 8;
          bf16x8 bw = cat8(lds_tr1(wa), lds_tr1(wa + 4 * 160));
          unsigned aa = scr + (db * 32 + fr) * 128 + (((ks * 4 + g) ^ ((fr >> 1) & 7)) << 4);
          d0 = mfma16(lds_b128(aa), bw, d0);
          d1 = mfma16(lds_b128(aa + 2048), bw, d1);
        }
        int kc = db * 4 + g;
        if (unit < 10 && kc < 5) {
          u32x4 o = {pack_bf16(d0[0], d0[1]), pack_bf16(d0[2], d0[3]), pack_bf16(d1[0], d1[1]), pack_bf16(d1[2], d1[3])};
          store16_sc1(p.ws + WS_B3, ((mcb * 176 + (b >> 3) * 5 + kc) * 80 + nf * 16 + fr) * 16, o);
        }
      }
      WAIT_LGKM0();
      SB(1);
    }
    constexpr int PST = 640 + 16;
    {
      unsigned a = scr + (st >> 4) * PST + (st & 15) * 40;
#pragma unroll
      for (int q = 0; q < 5; ++q) *(LAS u32x2*)(uintptr_t)(a + q * 8) = qw[q];
    }
    WAIT_LGKM0();
    SB(1);
    {
      const int prg = b >> 3, r0 = ((b >> 1) & 3) * 16;
#pragma unroll
      for (int it = 0; it < 3; ++it) {
        int idx = st + it * 256;
        if (idx < 640) {
          int ktl = idx >> 7, row = (idx >> 3) & 15, cp = idx & 7;
          int c = cp ^ (((r0 + row) >> 1) & 7);
          u32x4 v = *(LAS u32x4*)(uintptr_t)(scr + row * PST + (ktl * 8 + c) * 16);
          store16_sc1(p.ws + WS_UA, ((prg * 22 + (b & 1) * 5 + ktl) * 64 + r0 + row) * 128 + cp * 16, v);
        }
      }
    }
    SB(5);
    WAIT_VM(0);
    SB(1);
    FLAG_ADD_S(512 + (b & 15) * 32);
    SB(12);
    if (sw == 0) wait_flags(flags + 512, 16, 16u, lane);
    unsigned ul[3], ug[3];
    {
      constexpr int OST = 96 * 2 + 16;
#pragma unroll
      for (int it = 0; it < 3; ++it) {
        int idx = st + it * 256;
        int row = idx / CPR, c = idx - row * CPR;
        int cg = cb * 12 + c;
        ul[it] = pinv(scr + row * OST + c * 16);
        ug[it] = pinv(((rg * 22 + 10 + (cg >> 3)) * 64 + row) * 128 + (((cg & 7) ^ ((row >> 1) & 7)) << 4));
      }
    }
    SB(7);
    {
      u32x4 v[3];
#pragma unroll
      for (int it = 0; it < 3; ++it) v[it] = *(LAS u32x4*)(uintptr_t)ul[it];
#pragma unroll
      for (int it = 0; it < 3; ++it) store16_sc1(p.ws + WS_UA, ug[it], v[it]);
    }
    SB(2);
    WAIT_VM(0);
    SB(1);
    FLAG_ADD_S(1024 + rg * 32);
    SB(6);
    if (sw == 0) wait_flags(flags + 1024 + rg3 * 32, 1, 8u, lane);
    SB(1);
  } else if (wid >= 8) {
    __builtin_amdgcn_s_setprio(3);
    const int lw = wid - 8, lt = tid - 512;
    unsigned aoff1, aoff2, boff1[3];
    {
      int row = lw * 8 + (lane >> 3), c = (lane & 7) ^ ((row >> 1) & 7);
      aoff1 = ((rg * 64 + row) * D_ + c * 8) * 2;
      aoff2 = ((rg * 64 + row) * S_ + c * 8) * 2;
    }
#pragma unroll
    for (int i = 0; i < 3; ++i) {
      int slot = i * 256 + lt;
      int k = slot / CPR;
      boff1[i] = (k * (2 * F_) + cb * 96 + bswz<false>(k, slot - k * CPR) * 8) * 2;
    }
    const unsigned boff2 = lt * 16;
    const u16* zpanel = (const u16*)(p.ws + WS_Z2N) + (size_t)cb * S_ * 96;
    const u16* bpanel = (const u16*)(p.ws + WS_B3) + (size_t)cb3 * KA_ * 80;
    const u16* ua = (const u16*)(p.ws + WS_UA) + (size_t)rg3 * 22 * 4096;
    const bool b3 = lw < 2;
    const unsigned lbase = lds0 + lw * 1024;
    auto ld_z1_a = [&](int t, int sl) {
#pragma unroll
      for (int i = 0; i < 2; ++i) glds16(p.x + t * 64 + i * 32 * D_, aoff1, lbase + sl * K2_SLOT + i * 4096);
    };
    auto ld_z1_b = [&](int t, int sl) {
#pragma unroll
      for (int i = 0; i < 3; ++i) glds16(p.w_in + (size_t)t * 64 * (2 * F_), boff1[i], lbase + sl * K2_SLOT + 8192 + i * 4096);
    };
    auto ld_g_a = [&](int t, int sl) {
#pragma unroll
      for (int i = 0; i < 2; ++i) glds16(p.w_sp + t * 64 + i * 32 * S_, aoff2, lbase + sl * K2_SLOT + i * 4096);
    };
    auto ld_g_b = [&](int t, int sl) {
#pragma unroll
      for (int i = 0; i < 3; ++i) glds16_sc1(zpanel + (size_t)t * 64 * 96 + i * 2048, boff2, lbase + sl * K2_SLOT + 8192 + i * 4096);
    };
    auto ld_o_a = [&](int t, int sl) {
#pragma unroll
      for (int i = 0; i < 2; ++i) glds16_sc1(ua + t * 4096 + i * 2048, boff2, lbase + sl * K2_SLOT + i * 4096);
    };
    auto ld_o_b = [&](int t, int sl) {
#pragma unroll
      for (int i = 0; i < 2; ++i) glds16_sc1(bpanel + (size_t)t * 64 * 80 + i * 2048, boff2, lbase + sl * K2_SLOT + 8192 + i * 4096);
      if (b3) glds16_sc1(bpanel + (size_t)t * 64 * 80 + 4096, boff2, lbase + sl * K2_SLOT + 8192 + 8192);
    };
    int sl = 0;
    auto nx = [&]() { sl = sl == K2_NS - 1 ? 0 : sl + 1; };
#pragma unroll 1
    for (int t = 0; t < 2; ++t) { ld_z1_a(t, sl); ld_z1_b(t, sl); nx(); }
    WAIT_VM(5); BARRIER();
#pragma unroll 1
    for (int t = 2; t < 4; ++t) { ld_z1_a(t, sl); ld_z1_b(t, sl); nx(); }
    WAIT_VM(10); BARRIER();
#pragma unroll 1
    for (int t = 4; t < 6; ++t) { ld_z1_a(t, sl); ld_z1_b(t, sl); nx(); }
#pragma unroll 1
    for (int t = 6; t < 10; ++t) { ld_z1_a(t, sl); ld_z1_b(t, sl); nx(); WAIT_VM(20); BARRIER(); }
    ld_g_a(0, 3); WAIT_VM(17); BARRIER();
    ld_g_a(1, 4); WAIT_VM(14); BARRIER();
    ld_g_b(0, 3); ld_g_b(1, 4);
    sl = 5;
#pragma unroll 1
    for (int t = 2; t < 32; ++t) {
      ld_g_a(t, sl); ld_g_b(t, sl); nx();
      if (t == 4) WAIT_VM(18); else WAIT_VM(20);
      BARRIER();
    }
    ld_o_a(0, sl); ld_o_b(0, sl); nx(); if (b3) WAIT_VM(20); else WAIT_VM(19); BARRIER();
    ld_o_a(1, sl); ld_o_b(1, sl); nx(); if (b3) WAIT_VM(20); else WAIT_VM(18); BARRIER();
    ld_o_a(2, sl); ld_o_b(2, sl); nx(); if (b3) WAIT_VM(20); else WAIT_VM(17); BARRIER();
#pragma unroll 1
    for (int t = 3; t < 10; ++t) {
      ld_o_a(t, sl); ld_o_b(t, sl); nx();
      if (b3) WAIT_VM(20); else WAIT_VM(16);
      BARRIER();
    }
    const int sl52 = sl;
    ld_o_b(10, sl); nx(); if (b3) WAIT_VM(18); else WAIT_VM(14); BARRIER();
    ld_o_b(11, sl); nx(); if (b3) WAIT_VM(16); else WAIT_VM(12); BARRIER();
    ld_o_b(12, sl); nx(); if (b3) WAIT_VM(14); else WAIT_VM(10); BARRIER();
    ld_o_b(13, sl); nx(); if (b3) WAIT_VM(12); else WAIT_VM(8); BARRIER();
    ld_o_b(14, sl); nx(); BARRIER();
    ld_o_b(15, sl); nx(); BARRIER();
    {
      int s2 = sl52;
#pragma unroll 1
      for (int t = 10; t < 16; ++t) { ld_o_a(t, s2); s2 = s2 == K2_NS - 1 ? 0 : s2 + 1; }
    }
    WAIT_VM(10); BARRIER();
    WAIT_VM(8); BARRIER();
    ld_o_a(16, sl); ld_o_b(16, sl); nx(); if (b3) WAIT_VM(11); else WAIT_VM(10); BARRIER();
    ld_o_a(17, sl); ld_o_b(17, sl); nx(); if (b3) WAIT_VM(14); else WAIT_VM(12); BARRIER();
    ld_o_a(18, sl); ld_o_b(18, sl); nx(); if (b3) WAIT_VM(17); else WAIT_VM(14); BARRIER();
#pragma unroll 1
    for (int t = 19; t < 22; ++t) {
      ld_o_a(t, sl); ld_o_b(t, sl); nx();
      if (b3) WAIT_VM(20); else WAIT_VM(16);
      BARRIER();
    }
    if (b3) WAIT_VM(15); else WAIT_VM(12); BARRIER();
    if (b3) WAIT_VM(10); else WAIT_VM(8); BARRIER();
    if (b3) WAIT_VM(5); else WAIT_VM(4); BARRIER();
    WAIT_VM(0); BARRIER();
    BARRIER();
    BARRIER();
  } else {
    const int wr = wid >> 1, wc = wid & 1;
    const int kh = wid >> 2, wm = wid & 3;
    const int mcb = b & 7;
    const unsigned scr = lds0 + K2_SCR;
    LAS float* sx = (LAS float*)(uintptr_t)scr;
    LAS float* cst = (LAS float*)(uintptr_t)(scr + 16384);
    LAS float* krp = (LAS float*)(uintptr_t)(scr + 16384);
    const int urow = wr * 16 + fr;

    constexpr int ZST = 1536 + 32;
    u32x2 yv[3];
    u32x4 tb0;
    {
      unsigned oy = ((b * 8 + wid) * F_ + lane * 4) * 2;
#pragma unroll
      for (int i = 0; i < 3; ++i) asm volatile("global_load_dwordx2 %0, %1, %2" : "=v"(yv[i]) : "v"(oy + i * 512), "s"(p.ws + WS_Y2));
      unsigned ok = (((tid >> 4) & 15) * 13440 + (b < 210 ? b : 0) * 64 + (tid & 15) * 4) * 4;
      asm volatile("global_load_dwordx4 %0, %1, %2" : "=v"(tb0) : "v"(ok), "s"(p.ws + WS_KVP));
    }
    auto parta_compute = [&]() {
      asm volatile("s_waitcnt vmcnt(0)" : "+v"(yv[0]), "+v"(yv[1]), "+v"(yv[2]), "+v"(tb0) :: "memory");
      float z[12];
#pragma unroll
      for (int i = 0; i < 3; ++i)
#pragma unroll
        for (int q = 0; q < 2; ++q) {
          unsigned w = pack_bf16(gelu_f(bf_lo(yv[i][q])), gelu_f(bf_hi(yv[i][q])));
          z[i * 4 + q * 2] = bf_lo(w);
          z[i * 4 + q * 2 + 1] = bf_hi(w);
        }
      float s = 0.f;
#pragma unroll
      for (int i = 0; i < 12; ++i) s += z[i];
      s = wave_sum(s);
      float mean = s * (1.f / F_), v2 = 0.f;
#pragma unroll
      for (int i = 0; i < 12; ++i) { z[i] -= mean; v2 = __builtin_fmaf(z[i], z[i], v2); }
      v2 = wave_sum(v2);
      float rstd = __builtin_amdgcn_rsqf(v2 * (1.f / F_) + 1e-5f);
#pragma unroll
      for (int i = 0; i < 3; ++i)
        *(LAS u32x2*)(uintptr_t)(scr + wid * ZST + (i * 256 + lane * 4) * 2) =
            u32x2{pack_bf16(z[i * 4] * rstd, z[i * 4 + 1] * rstd), pack_bf16(z[i * 4 + 2] * rstd, z[i * 4 + 3] * rstd)};
      if (wid < 4) *(LAS u32x4*)(krp + tid * 4) = tb0;
      WAIT_LGKM0();
    };
    auto parta_store = [&]() {
#pragma unroll
      for (int it = 0; it < 2; ++it) {
        int grp64 = it ? 8 + (wid & 3) : wid;
        int q = fr >> 2, pp = fr & 3;
        unsigned a = scr + q * ZST + (grp64 * 64 + g * 16 + pp * 4) * 2;
        bf16x8 v = cat8(lds_tr1(a), lds_tr1(a + 4 * ZST));
        int f = grp64 * 64 + lane, c = f / 96, n = f - c * 96;
        if (it == 0 || wid < 4) store16_sc1(p.ws + WS_Z2N, ((c * 256 + b) * 96 + n) * 16, __builtin_bit_cast(u32x4, v));
      }
      if (tid >= 448 && tid < 464 && b < 210) {
        int v = tid - 448;
        f32x4 t4 = {0, 0, 0, 0};
#pragma unroll
        for (int q = 0; q < 16; ++q) t4 += *(LAS f32x4*)(krp + q * 64 + v * 4);
        store16_sc1(p.ws + WS_KVR, (b * 64 + v * 4) * 4, __builtin_bit_cast(u32x4, t4));
      }
    };

    unsigned abase[2], baddr1[3], baddr2, abase3, baddr3;
    {
      int x7 = (fr >> 1) & 7;
#pragma unroll
      for (int ks = 0; ks < 2; ++ks) abase[ks] = lds0 + (wr * 16 + fr) * 128 + (((ks * 4 + g) ^ x7) << 4);
      int q = fr >> 2, pp = fr & 3;
#pragma unroll
      for (int n = 0; n < 3; ++n) {
        int k = 8 * g + q;
        baddr1[n] = lds0 + 8192 + (k * CPR + bswz<false>(k, 2 * (wc * 3 + n) + (pp >> 1))) * 16 + (pp & 1) * 8;
      }
      baddr2 = lds0 + 8192 + (g * 96 + wc * 48 + fr) * 16;
      abase3 = lds0 + (wm * 16 + fr) * 128 + (((kh * 4 + g) ^ x7) << 4);
      baddr3 = lds0 + 8192 + ((kh * 4 + g) * 80 + fr) * 16;
    }
    f32x4 acc1[3], acc2[3], cs[3], acc3[5];
#pragma unroll
    for (int n = 0; n < 3; ++n) { acc1[n] = f32x4{0, 0, 0, 0}; acc2[n] = f32x4{0, 0, 0, 0}; cs[n] = f32x4{0, 0, 0, 0}; }
#pragma unroll
    for (int n = 0; n < 5; ++n) acc3[n] = f32x4{0, 0, 0, 0};
    const short one = 0x3f80;
    bf16x8 ones = {one, one, one, one, one, one, one, one};
    asm volatile("" : "+v"(ones));

    bf16x8 fA[8], fB[8];
    unsigned slot = 0;
    int z1t = 0;
    auto adv = [&]() { slot = slot == (K2_NS - 1) * K2_SLOT ? 0 : slot + K2_SLOT; };
    auto ldf = [&](bf16x8 (&f)[8], int mode, int i, unsigned& tmp) {
      if (mode == 2) {
        if (i == 0) f[0] = lds_b128(pinv(abase3 + slot));
        else if (i < 6) { if (i == 1) tmp = pinv(baddr3 + slot); f[1 + i] = lds_b128(tmp + (i - 1) * 256); }
      } else {
        if (i == 0 || i == 4) f[i >> 2] = lds_b128(pinv(abase[i >> 2] + slot));
        else {
          int ks = i >> 2, n = (i & 3) - 1;
          if (mode == 0) {
            unsigned a = pinv(baddr1[n] + slot) + ks * 32 * CPR * 16;
            f[2 + ks * 3 + n] = cat8(lds_tr1(a), lds_tr1(a + 4 * CPR * 16));
          } else {
            if (i == 1) tmp = pinv(baddr2 + slot);
            f[2 + ks * 3 + n] = lds_b128(tmp + ks * 4 * 96 * 16 + n * 256);
          }
        }
      }
    };
    auto iter = [&](bf16x8 (&fc)[8], bf16x8 (&fn)[8], auto mc, auto mn) {
      constexpr int MC = decltype(mc)::value, MN = decltype(mn)::value;
      unsigned tmp = 0;
#pragma unroll
      for (int i = 0; i < 8; ++i) {
        if (MC == 2) {
          if (i < 5) acc3[i] = mfma16(fc[2 + i], fc[0], acc3[i]);
        } else if (i < 6) {
          int ks = i / 3, n = i % 3;
          if (MC == 0) acc1[n] = mfma16(fc[2 + i], fc[ks], acc1[n]);
          else acc2[n] = mfma16(fc[2 + i], fc[ks], acc2[n]);
        }
        if (MN < 3) ldf(fn, MN, i, tmp);
        SBAR();
      }
      if (MC == 0) {
        if (wr == (z1t & 3)) {
#pragma unroll
          for (int i = 0; i < 6; ++i) cs[i % 3] = mfma16(fc[2 + i], ones, cs[i % 3]);
        }
        ++z1t;
        SBAR();
      }
      if (MN < 3) adv();
    };
    using I0 = std::integral_constant<int, 0>;
    using I1 = std::integral_constant<int, 1>;
    using I2 = std::integral_constant<int, 2>;
    using I3 = std::integral_constant<int, 3>;
#define FLAG_ADD(off) do { if (tid == 0) __hip_atomic_fetch_add((gu32*)(flags + (off)), 1u, __ATOMIC_RELAXED, __HIP_MEMORY_SCOPE_AGENT); } while (0)
    const f32x2 xs = *(const f32x2*)((const float*)(p.ws + WS_XST) + (size_t)(rg * 64 + urow) * 2);
    const float bias = bf_lo(p.b_sp[rg * 64 + urow]);
    parta_compute();
    BARRIER();
    parta_store();
    {
      unsigned tmp = 0;
#pragma unroll
      for (int i = 0; i < 8; ++i) ldf(fA, 0, i, tmp);
      adv();
    }
    BARRIER();
    iter(fA, fB, I0{}, I0{}); WAIT_VM(0); BARRIER();
    FLAG_ADD((b & 15) * 32);
    iter(fB, fA, I0{}, I0{}); BARRIER();
    iter(fA, fB, I0{}, I0{}); BARRIER();
    iter(fB, fA, I0{}, I0{}); BARRIER();
    iter(fA, fB, I0{}, I0{}); BARRIER();
    iter(fB, fA, I0{}, I0{}); BARRIER();
    iter(fA, fB, I0{}, I0{}); BARRIER();
    iter(fB, fA, I0{}, I0{}); BARRIER();
    iter(fA, fB, I0{}, I0{}); BARRIER();
    iter(fB, fA, I0{}, I1{}); BARRIER();
#pragma unroll 1
    for (int t = 10; t < 18; t += 2) {
      iter(fA, fB, I1{}, I1{}); BARRIER();
      iter(fB, fA, I1{}, I1{}); BARRIER();
    }
    iter(fA, fB, I1{}, I1{});
    if (fr == 0) {
#pragma unroll
      for (int n = 0; n < 3; ++n) *(LAS f32x4*)(cst + wr * 96 + wc * 48 + n * 16 + g * 4) = cs[n];
    }
    WAIT_LGKM0();
    BARRIER();
    auto z1act = [&](int n) {
      SBAR();
      int c0 = wc * 48 + n * 16 + g * 4;
      f32x4 c4 = (*(LAS f32x4*)(cst + c0) + *(LAS f32x4*)(cst + 96 + c0)) + (*(LAS f32x4*)(cst + 192 + c0) + *(LAS f32x4*)(cst + 288 + c0));
#pragma unroll
      for (int r = 0; r < 4; ++r) acc1[n][r] = gelu_f(__builtin_fmaf(acc1[n][r], xs[1], c4[r] * xs[0]));
      asm volatile("" : "+v"(acc1[n]));
      SBAR();
    };
    iter(fB, fA, I1{}, I1{}); z1act(0); BARRIER();
    iter(fA, fB, I1{}, I1{}); z1act(1); BARRIER();
    iter(fB, fA, I1{}, I1{}); z1act(2); BARRIER();
    iter(fA, fB, I1{}, I1{}); BARRIER();
    u32x4 xv0, xv1;
    unsigned go0, go1, la0, la1;
    {
      int i0 = tid, i1 = tid < 128 ? tid + 512 : tid;
      go0 = pinv((rg3 * 64 + i0 / 10) * D_ + cb3 * 80 + (i0 % 10) * 8);
      go1 = pinv((rg3 * 64 + i1 / 10) * D_ + cb3 * 80 + (i1 % 10) * 8);
      la0 = pinv(lds0 + (i0 / 10) * (80 * 4 + 16) + (i0 % 10) * 32);
      la1 = pinv(lds0 + (i1 / 10) * (80 * 4 + 16) + (i1 % 10) * 32);
      xv0 = *(const u32x4*)(p.x + go0);
      xv1 = *(const u32x4*)(p.x + go1);
    }
    iter(fB, fA, I1{}, I1{}); BARRIER();
#pragma unroll 1
    for (int t = 24; t < 34; t += 2) {
      iter(fA, fB, I1{}, I1{}); BARRIER();
      iter(fB, fA, I1{}, I1{}); BARRIER();
    }
    iter(fA, fB, I1{}, I1{}); BARRIER();
    iter(fB, fA, I1{}, I1{}); BARRIER();
#pragma unroll 1
    for (int t = 36; t < 40; t += 2) {
      iter(fA, fB, I1{}, I1{}); BARRIER();
      iter(fB, fA, I1{}, I1{}); BARRIER();
    }
    iter(fA, fB, I1{}, I1{}); BARRIER();
    iter(fB, fA, I1{}, I2{});
    constexpr int OST = 96 * 2 + 16;
    {
      unsigned dst = scr + urow * OST + (wc * 48 + g * 4) * 2;
#pragma unroll
      for (int n = 0; n < 3; ++n) {
        float v[4];
#pragma unroll
        for (int r = 0; r < 4; ++r) v[r] = acc1[n][r] * (acc2[n][r] + bias);
        *(LAS u32x2*)(uintptr_t)(dst + n * 32) = u32x2{pack_bf16(v[0], v[1]), pack_bf16(v[2], v[3])};
      }
    }
    SBAR();
    WAIT_LGKM0();
    BARRIER();
    iter(fA, fB, I2{}, I2{}); BARRIER();
    iter(fB, fA, I2{}, I2{}); BARRIER();
    iter(fA, fB, I2{}, I2{}); BARRIER();
    iter(fB, fA, I2{}, I2{}); BARRIER();
#pragma unroll 1
    for (int t = 46; t < 50; t += 2) {
      iter(fA, fB, I2{}, I2{}); BARRIER();
      iter(fB, fA, I2{}, I2{}); BARRIER();
    }
    iter(fA, fB, I2{}, I2{}); BARRIER();
    iter(fB, fA, I2{}, I3{});
    BARRIER();
    BARRIER();
    {
      unsigned tmp = 0;
#pragma unroll
      for (int i = 0; i < 8; ++i) ldf(fA, 2, i, tmp);
      adv();
    }
    BARRIER();
#pragma unroll 1
    for (int t = T_B; t < T_END; t += 2) {
      iter(fA, fB, I2{}, I2{}); BARRIER();
      iter(fB, fA, I2{}, I2{});
      if (t == T_END - 2) WAIT_LGKM0();
      BARRIER();
    }
    constexpr int OS3 = 80 * 4 + 16;
    {
      unsigned dst = lds0 + kh * (64 * OS3) + (wm * 16 + fr) * OS3 + g * 16;
#pragma unroll
      for (int n = 0; n < 5; ++n) *(LAS f32x4*)(uintptr_t)(dst + n * 64) = acc3[n];
    }
    WAIT_LGKM0();
    BARRIER();
#pragma unroll
    for (int it = 0; it < 2; ++it) {
      if (it == 0 || tid < 128) {
        u32x4 xv = it ? xv1 : xv0;
        unsigned la = it ? la1 : la0;
        f32x4 a = *(LAS f32x4*)(uintptr_t)(la) + *(LAS f32x4*)(uintptr_t)(la + 64 * OS3);
        f32x4 c4 = *(LAS f32x4*)(uintptr_t)(la + 16) + *(LAS f32x4*)(uintptr_t)(la + 64 * OS3 + 16);
        u32x4 o = {pack_bf16(bf_lo(xv[0]) + a[0], bf_hi(xv[0]) + a[1]), pack_bf16(bf_lo(xv[1]) + a[2], bf_hi(xv[1]) + a[3]),
                   pack_bf16(bf_lo(xv[2]) + c4[0], bf_hi(xv[2]) + c4[1]), pack_bf16(bf_lo(xv[3]) + c4[2], bf_hi(xv[3]) + c4[3])};
        *(u32x4*)(p.out + (it ? go1 : go0)) = o;
      }
    }
  }
}

extern "C" void kernel_launch(void* const* d_in, const int* in_sizes, int n_in, void* d_out, int out_size, void* d_ws,
                              size_t ws_size, hipStream_t stream) {
  Params p;
  p.x = (const u16*)d_in[0];
  p.wqkv = (const u16*)d_in[1];
  p.wo = (const u16*)d_in[2];
  p.w_in = (const u16*)d_in[4];
  p.w_sp = (const u16*)d_in[5];
  p.b_sp = (const u16*)d_in[6];
  p.w_out = (const u16*)d_in[7];
  p.out = (u16*)d_out;
  p.ws = (char*)d_ws;
  (void)hipFuncSetAttribute((const void*)stage1_kernel, hipFuncAttributeMaxDynamicSharedMemorySize, S1_LDS);
  (void)hipFuncSetAttribute((const void*)stage2_kernel, hipFuncAttributeMaxDynamicSharedMemorySize, K2_LDS);
  stage1_kernel<<<256, S1_THREADS, S1_LDS, stream>>>(p);
  stage2_kernel<<<256, K2_THREADS, K2_LDS, stream>>>(p);
}

#if defined(__HIP_DEVICE_COMPILE__) && __HIP_DEVICE_COMPILE__
#pragma clang attribute pop
#endif
```

```cpp
#if defined(__HIP_DEVICE_COMPILE__) && __HIP_DEVICE_COMPILE__
#pragma clang attribute push(__attribute__((target("no-packed-fp32-ops"))), apply_to = function)
#endif
#include <hip/hip_runtime.h>
#include <hip/hip_bf16.h>
#include <stdint.h>
#include <type_traits>

typedef short bf16x8 __attribute__((ext_vector_type(8)));
typedef short s16x4 __attribute__((ext_vector_type(4)));
typedef float f32x4 __attribute__((ext_vector_type(4)));
typedef float f32x2 __attribute__((ext_vector_type(2)));
typedef unsigned u32x4 __attribute__((ext_vector_type(4)));
typedef unsigned u32x2 __attribute__((ext_vector_type(2)));
typedef __bf16 bf16x2_t __attribute__((ext_vector_type(2)));
typedef unsigned short u16;

#define TIDX ((int)__builtin_amdgcn_workitem_id_x())
#define BIDX ((int)__builtin_amdgcn_workgroup_id_x())
#define LAS __attribute__((address_space(3)))
#define DI __device__ __forceinline__

constexpr int S_ = 2048, D_ = 640, H_ = 32, DH_ = 20, F_ = 768, KA_ = D_ + F_;

constexpr size_t WS_Y2 = 0;
constexpr size_t WS_Q = WS_Y2 + (size_t)S_ * F_ * 2;
constexpr size_t WS_UA = WS_Q + (size_t)S_ * D_ * 2;
constexpr size_t WS_B3 = WS_UA + (size_t)S_ * KA_ * 2;
constexpr size_t WS_KVP = WS_B3 + (size_t)KA_ * D_ * 2;
constexpr size_t WS_XST = WS_KVP + (size_t)16 * 32 * 420 * 4;
constexpr size_t WS_Z2N = WS_XST + (size_t)S_ * 2 * 4;
constexpr size_t WS_END = WS_Z2N + (size_t)S_ * F_ * 2;
constexpr size_t WS_FLAGS = WS_END;
constexpr size_t WS_KVR = WS_FLAGS + 64 * 32 * 4;

DI unsigned pack_bf16(float a, float b) {
  f32x2 v = {a, b};
  bf16x2_t r = __builtin_convertvector(v, bf16x2_t);
  return __builtin_bit_cast(unsigned, r);
}
DI float bf_lo(unsigned w) { return __builtin_bit_cast(float, w << 16); }
DI float bf_hi(unsigned w) { return __builtin_bit_cast(float, w & 0xffff0000u); }
DI float phi_f(float t) {
  return (fmaxf(t, 0.f) + 1e-6f) + __builtin_amdgcn_exp2f(fminf(t, 0.f) * 1.4426950408889634f);
}
DI float gelu_f(float x) {
  constexpr float c1 = -2.f * 0.7978845608028654f * 1.4426950408889634f;
  constexpr float c3 = c1 * 0.044715f;
  float a = x * __builtin_fmaf(x * x, c3, c1);
  return x * __builtin_amdgcn_rcpf(1.f + __builtin_amdgcn_exp2f(a));
}
DI float wave_sum(float v) {
  v += __builtin_bit_cast(float, __builtin_amdgcn_update_dpp(0, __builtin_bit_cast(int, v), 0xB1, 0xf, 0xf, false));
  v += __builtin_bit_cast(float, __builtin_amdgcn_update_dpp(0, __builtin_bit_cast(int, v), 0x4E, 0xf, 0xf, false));
  v += __builtin_bit_cast(float, __builtin_amdgcn_update_dpp(0, __builtin_bit_cast(int, v), 0x141, 0xf, 0xf, false));
  v += __builtin_bit_cast(float, __builtin_amdgcn_update_dpp(0, __builtin_bit_cast(int, v), 0x140, 0xf, 0xf, false));
  int iv = __builtin_bit_cast(int, v);
  float r0 = __builtin_bit_cast(float, __builtin_amdgcn_readlane(iv, 0)), r1 = __builtin_bit_cast(float, __builtin_amdgcn_readlane(iv, 16));
  float r2 = __builtin_bit_cast(float, __builtin_amdgcn_readlane(iv, 32)), r3 = __builtin_bit_cast(float, __builtin_amdgcn_readlane(iv, 48));
  return (r0 + r1) + (r2 + r3);
}
DI f32x4 mfma16(bf16x8 a, bf16x8 b, f32x4 c) { return __builtin_amdgcn_mfma_f32_16x16x32_bf16(a, b, c, 0, 0, 0); }
DI bf16x8 lds_b128(unsigned addr) { return *(LAS bf16x8*)(uintptr_t)addr; }
DI s16x4 lds_tr1(unsigned a) { return __builtin_amdgcn_ds_read_tr16_b64_v4i16((LAS s16x4*)(uintptr_t)a); }
DI bf16x8 cat8(s16x4 lo, s16x4 hi) { return __builtin_shufflevector(lo, hi, 0, 1, 2, 3, 4, 5, 6, 7); }
DI void glds16(const void* sbase, unsigned voff, unsigned lds_addr) {
  unsigned sv;
  asm volatile("s_mov_b32 %0, m0\n\ts_mov_b32 m0, %3\n\ts_nop 0\n\tglobal_load_lds_dwordx4 %1, %2\n\ts_mov_b32 m0, %0"
               : "=&s"(sv) : "v"(voff), "s"(sbase), "s"(lds_addr));
}
DI void touch_line(unsigned& sink, const void* sbase, unsigned voff) {
  asm volatile("global_load_dword %0, %1, %2" : "=v"(sink) : "v"(voff), "s"(sbase));
}
#define WAIT_VM(n) asm volatile("s_waitcnt vmcnt(%0)" ::"i"(n) : "memory")
#define WAIT_LGKM0() asm volatile("s_waitcnt lgkmcnt(0)" ::: "memory")
#define BARRIER() do { asm volatile("" ::: "memory"); __builtin_amdgcn_s_barrier(); asm volatile("" ::: "memory"); } while (0)
#define SBAR() __builtin_amdgcn_sched_barrier(0)
DI unsigned pinv(unsigned a) { asm volatile("" : "+v"(a)); return a; }

struct Params {
  const u16* x;
  const u16* wqkv;
  const u16* wo;
  const u16* w_in;
  const u16* w_sp;
  const u16* b_sp;
  const u16* w_out;
  u16* out;
  char* ws;
};

template <bool XQ>
DI int bswz(int k, int c) { return c ^ (((k >> 3) & 1) << 1) ^ (XQ ? (((k >> 1) & 1) << 2) : 0); }

constexpr int S1_STAGE = 16384 + 24576;
constexpr int S1_NBUF = 4;
constexpr int S1_LDS = S1_NBUF * S1_STAGE;
constexpr int S1_THREADS = 768;

template <int TYPE>
DI void stage1_body(const Params& p, int rg, int ct, unsigned lds0, int xcd, int jb) {
  constexpr int BN = TYPE == 2 ? 192 : 160;
  constexpr int CPR = BN / 8;
  constexpr int NF = BN / 32;
  constexpr int BI = BN / 32;
  constexpr int G = 4 + BI;
  constexpr int NT = D_ / 64;
  constexpr bool XQ = TYPE == 2;
  const int tid = TIDX, lane = tid & 63;
  const int wid = __builtin_amdgcn_readfirstlane(tid >> 6);
  const int fr = lane & 15, g = lane >> 4;
  const int wr = (wid >> 1) & 3, wc = wid & 1;

  LAS float* st_rs = (LAS float*)(uintptr_t)lds0;
  LAS float* st_sq = st_rs + 128;
  LAS float* st_cs = (LAS float*)(uintptr_t)(lds0 + 1024);
  const unsigned otile = lds0 + 4096;
  constexpr int OST = BN * 2 + 16;

  unsigned gofs[4] = {0, 0, 0, 0};
  if (TYPE == 0 && wid < 8) {
#pragma unroll
    for (int it = 0; it < 4; ++it) {
      int i = tid + it * 512;
      if (i >= 1680) i = 0;
      int hl = i / 420, w = i - hl * 420;
      int hi = hl & 1, ci, cj, q;
      if (w < 400) {
        int d = w / 20, e = w - d * 20;
        ci = hi * 20 + d; cj = 40 + hi * 20 + e;
        int pi = ci >> 4, pj = cj >> 4;
        q = pi == 0 ? pj - 2 : (pi == 1 ? pj : pj + 2);
      } else {
        ci = hi * 20 + (w - 400); cj = 0;
        q = 7 + (ci >> 4);
      }
      gofs[it] = pinv((hl >> 1) * 10240 + q * 1024 + ((((ci & 15) >> 2) * 16 + (cj & 15)) * 4 + (ci & 3)) * 4);
    }
  }

  if (wid >= 8) {
    const int lw = wid - 8;
    const u16* Bm = TYPE == 2 ? p.w_in : p.wqkv;
    constexpr int ldb = TYPE == 2 ? 2 * F_ : 3 * D_;
    unsigned aoff, boff[BI];
    {
      int row = lw * 8 + (lane >> 3), c = (lane & 7) ^ ((row >> 1) & 7);
      aoff = ((rg * 128 + row) * D_ + c * 8) * 2;
    }
#pragma unroll
    for (int i = 0; i < BI; ++i) {
      int slot = (i * 4 + lw) * 64 + lane;
      int k = slot / CPR, c = bswz<XQ>(k, slot - k * CPR);
      int col;
      if (TYPE == 0) col = c < 10 ? (D_ + ct * 80 + c * 8) : (2 * D_ + ct * 80 + (c - 10) * 8);
      else if (TYPE == 1) col = ct * 160 + c * 8;
      else col = F_ + ct * 192 + c * 8;
      boff[i] = (k * ldb + col) * 2;
    }
    const int rot = ((jb & 7) + 3 * (jb >> 3)) % NT;
    auto issue = [&](int t) {
      unsigned base = lds0 + (t & 3) * S1_STAGE;
      const int kt = t + rot >= NT ? t + rot - NT : t + rot;
#pragma unroll
      for (int i = 0; i < 4; ++i) glds16(p.x + kt * 64 + i * 32 * D_, aoff, base + (i * 4 + lw) * 1024);
#pragma unroll
      for (int i = 0; i < BI; ++i) glds16(Bm + (size_t)kt * 64 * ldb, boff[i], base + 16384 + (i * 4 + lw) * 1024);
    };
    issue(0);
    issue(1);
    unsigned sk[5];
    WAIT_VM(G);
    BARRIER();
    issue(2);
    WAIT_VM(G);
    BARRIER();
#pragma unroll 1
    for (int t = 0; t < NT - 4; ++t) {
      issue(t + 3);
      WAIT_VM(G);
      BARRIER();
    }
    issue(NT - 1);
    WAIT_VM(G);
    BARRIER();
    WAIT_VM(0);
    BARRIER();
    if (TYPE != 0) {
      const int lt = lw * 64 + lane;
      const int i16 = (TYPE - 1) * 8 + (jb & 7);
      const int i128 = xcd * 16 + i16;
      touch_line(sk[0], p.w_sp, ((((xcd & 3) * 2 + (xcd >> 2)) * 16 + i16) * 512 + lt) * 128);
      touch_line(sk[1], p.w_sp, ((((xcd & 3) * 2 + (xcd >> 2)) * 16 + i16) * 512 + 256 + lt) * 128);
      int i = lt < 60 ? lt : 0;
      touch_line(sk[2], p.w_in, (((xcd & 3) * 160 + i16 * 10 + i / 6) * (2 * F_) + (xcd >> 2) * 384) * 2 + (i % 6) * 128);
      touch_line(sk[3], p.w_out, (i128 * 60 + (lt < 60 ? lt : 0)) * 128);
      touch_line(sk[4], p.wo, (i128 * 50 + (lt < 50 ? lt : 0)) * 128);
    }
    BARRIER();
    BARRIER();
    BARRIER();
    BARRIER();
    if (TYPE != 0) asm volatile("s_waitcnt vmcnt(0)" : "+v"(sk[0]), "+v"(sk[1]), "+v"(sk[2]), "+v"(sk[3]), "+v"(sk[4]) :: "memory");
    return;
  } else {
    unsigned abase[2], baddr[NF];
    {
      int x7 = (fr >> 1) & 7;
#pragma unroll
      for (int ks = 0; ks < 2; ++ks) abase[ks] = lds0 + (wr * 32 + wc * 16 + fr) * 128 + (((ks * 4 + g) ^ x7) << 4);
      int q = fr >> 2, pp = fr & 3;
#pragma unroll
      for (int n = 0; n < NF; ++n) {
        int c;
        if (TYPE == 0) {
          int w = 2 * n + (pp >> 1);
          c = w < 5 ? wc * 5 + w : 10 + wc * 5 + (w - 5);
        } else {
          c = 2 * (wc * NF + n) + (pp >> 1);
        }
        int k = 8 * g + q;
        baddr[n] = lds0 + 16384 + (k * CPR + bswz<XQ>(k, c)) * 16 + (pp & 1) * 8;
      }
    }
    const int am1 = wc ? -2048 : 2048;
    f32x4 acc[2][NF], cs[NF], rs, sq;
#pragma unroll
    for (int m = 0; m < 2; ++m)
#pragma unroll
      for (int n = 0; n < NF; ++n) acc[m][n] = f32x4{0, 0, 0, 0};
#pragma unroll
    for (int n = 0; n < NF; ++n) cs[n] = f32x4{0, 0, 0, 0};
    rs = f32x4{0, 0, 0, 0};
    sq = f32x4{0, 0, 0, 0};
    const short one = 0x3f80;
    bf16x8 ones = {one, one, one, one, one, one, one, one};
    asm volatile("" : "+v"(ones));

    bf16x8 af0[2], af1[2];
    s16x4 bl0[NF], bh0[NF], bl1[NF], bh1[NF];
    auto phase = [&](bf16x8 (&afc)[2], s16x4 (&blc)[NF], s16x4 (&bhc)[NF], bf16x8 (&afn)[2], s16x4 (&bln)[NF], s16x4 (&bhn)[NF],
                     unsigned sbn, int ksn, bool docs) {
      const unsigned a0 = pinv(abase[ksn] + sbn);
      unsigned ba = 0;
#pragma unroll
      for (int i = 0; i < 2 * NF + 2; ++i) {
        if (i < 2 * NF) {
          int m = i / NF, n = i % NF;
          bf16x8 bq = cat8(blc[n], bhc[n]);
          acc[m][n] = TYPE == 0 ? mfma16(afc[m], bq, acc[m][n]) : mfma16(bq, afc[m], acc[m][n]);
        } else if (i == 2 * NF) rs = mfma16(afc[0], ones, rs);
        else sq = mfma16(afc[0], afc[0], sq);
        if (i == 0) afn[0] = lds_b128(a0);
        else if (i <= 2 * NF) {
          int r = i - 1;
          if (r & 1) bhn[r >> 1] = lds_tr1(ba + (ksn * 32 + 4) * CPR * 16);
          else { ba = pinv(baddr[r >> 1] + sbn); bln[r >> 1] = lds_tr1(ba + ksn * 32 * CPR * 16); }
        } else afn[1] = lds_b128(a0 + am1);
        SBAR();
      }
      if (docs) {
#pragma unroll
        for (int n = 0; n < NF; ++n) cs[n] = mfma16(ones, cat8(blc[n], bhc[n]), cs[n]);
      }
      SBAR();
    };
    BARRIER();
    af0[0] = lds_b128(abase[0]);
    af0[1] = lds_b128(abase[0] + am1);
#pragma unroll
    for (int n = 0; n < NF; ++n) { bl0[n] = lds_tr1(baddr[n]); bh0[n] = lds_tr1(baddr[n] + 4 * CPR * 16); }
    phase(af0, bl0, bh0, af1, bl1, bh1, 0, 1, wr == 0);
    BARRIER();
    phase(af1, bl1, bh1, af0, bl0, bh0, S1_STAGE, 0, wr == 1);
    BARRIER();
#pragma unroll 1
    for (int t = 1; t < NT; ++t) {
      phase(af0, bl0, bh0, af1, bl1, bh1, (t & 3) * S1_STAGE, 1, wr == ((2 * t) & 3));
      phase(af1, bl1, bh1, af0, bl0, bh0, ((t + 1) & 3) * S1_STAGE, 0, wr == ((2 * t + 1) & 3));
      if (t == NT - 1) WAIT_LGKM0();
      BARRIER();
    }
    {
      int r0 = wr * 32 + wc * 16;
      if (fr == 0) *(LAS f32x4*)(st_rs + r0 + g * 4) = rs;
      if ((fr >> 2) == g) {
        int r = fr & 3;
        st_sq[r0 + fr] = r == 0 ? sq[0] : r == 1 ? sq[1] : r == 2 ? sq[2] : sq[3];
      }
      if (g == 0) {
#pragma unroll
        for (int n = 0; n < NF; ++n) st_cs[wr * 192 + wc * NF * 16 + n * 16 + fr] = cs[n][0];
      }
    }
    WAIT_LGKM0();
    BARRIER();
    if (TYPE == 0) {
      float csv[NF];
#pragma unroll
      for (int n = 0; n < NF; ++n) {
        int c0 = wc * NF * 16 + n * 16 + fr;
        csv[n] = (st_cs[c0] + st_cs[192 + c0]) + (st_cs[384 + c0] + st_cs[576 + c0]);
      }
      bf16x8 xf[NF];
#pragma unroll
      for (int m = 0; m < 2; ++m) {
        int r0 = wr * 32 + (m ^ wc) * 16 + g * 4;
        f32x4 mean = *(LAS f32x4*)(st_rs + r0) * (1.f / D_);
        f32x4 ex2 = *(LAS f32x4*)(st_sq + r0) * (1.f / D_);
        f32x4 rstd, nm;
#pragma unroll
        for (int r = 0; r < 4; ++r) {
          rstd[r] = __builtin_amdgcn_rsqf(fmaxf(ex2[r] - mean[r] * mean[r], 0.f) + 1e-5f);
          nm[r] = -mean[r] * rstd[r];
        }
#pragma unroll
        for (int n = 0; n < NF; ++n) {
          f32x4 y = acc[m][n] * rstd + nm * csv[n];
          bool isk = (n * 16 + fr) < 40;
          float v[4];
#pragma unroll
          for (int r = 0; r < 4; ++r) v[r] = n < 2 ? phi_f(y[r]) : (n == 2 ? (isk ? phi_f(y[r]) : y[r]) : y[r]);
          u32x4 t4 = __builtin_bit_cast(u32x4, xf[n]);
          t4[m * 2] = pack_bf16(v[0], v[1]);
          t4[m * 2 + 1] = pack_bf16(v[2], v[3]);
          xf[n] = __builtin_bit_cast(bf16x8, t4);
        }
      }
      SBAR();
      constexpr int PI[7] = {0, 0, 1, 1, 1, 2, 2};
      constexpr int PJ[7] = {2, 3, 2, 3, 4, 3, 4};
      f32x4 dd[10];
#pragma unroll
      for (int q = 0; q < 7; ++q) dd[q] = mfma16(xf[PI[q]], xf[PJ[q]], f32x4{0, 0, 0, 0});
#pragma unroll
      for (int i = 0; i < 3; ++i) dd[7 + i] = mfma16(xf[i], ones, f32x4{0, 0, 0, 0});
      SBAR();
#pragma unroll
      for (int q = 0; q < 10; ++q) *(LAS f32x4*)(uintptr_t)(otile + wid * 10240 + q * 1024 + lane * 16) = dd[q];
    } else {
      f32x4 csv[NF];
#pragma unroll
      for (int n = 0; n < NF; ++n) {
        int c0 = wc * NF * 16 + n * 16 + g * 4;
        csv[n] = (*(LAS f32x4*)(st_cs + c0) + *(LAS f32x4*)(st_cs + 192 + c0)) + (*(LAS f32x4*)(st_cs + 384 + c0) + *(LAS f32x4*)(st_cs + 576 + c0));
      }
#pragma unroll
      for (int m = 0; m < 2; ++m) {
        int rl = wr * 32 + (m ^ wc) * 16 + fr;
        float mean = st_rs[rl] * (1.f / D_);
        float ex2 = st_sq[rl] * (1.f / D_);
        float rstd = __builtin_amdgcn_rsqf(fmaxf(ex2 - mean * mean, 0.f) + 1e-5f);
        float nm = -mean * rstd;
        if (TYPE == 2 && ct == 0 && m == 0 && g == 0) *(f32x2*)((float*)(p.ws + WS_XST) + (size_t)(rg * 128 + rl) * 2) = f32x2{nm, rstd};
        unsigned dst = otile + rl * OST + (wc * NF * 16 + g * 4) * 2;
#pragma unroll
        for (int n = 0; n < NF; ++n) {
          f32x4 y = acc[m][n] * rstd + csv[n] * nm;
          *(LAS u32x2*)(uintptr_t)(dst + n * 32) = u32x2{pack_bf16(y[0], y[1]), pack_bf16(y[2], y[3])};
        }
      }
    }
  }
  WAIT_LGKM0();
  BARRIER();
  if (TYPE == 0) {
    float* kvp = (float*)(p.ws + WS_KVP) + ((size_t)rg * 32 + ct * 4) * 420;
#pragma unroll
    for (int it = 0; it < 4; ++it) {
      int i = tid + it * 512;
      if (i < 1680) {
        unsigned a = otile + gofs[it];
        float v = (*(LAS float*)(uintptr_t)(a) + *(LAS float*)(uintptr_t)(a + 2 * 10240)) +
                  (*(LAS float*)(uintptr_t)(a + 4 * 10240) + *(LAS float*)(uintptr_t)(a + 6 * 10240));
        kvp[i] = v;
      }
    }
  } else {
    u16* dstm = TYPE == 1 ? (u16*)(p.ws + WS_Q) + (size_t)rg * 128 * D_ + ct * 160
                          : (u16*)(p.ws + WS_Y2) + (size_t)rg * 128 * F_ + ct * 192;
    constexpr int ldo = TYPE == 1 ? D_ : F_;
    for (int idx = tid; idx < 128 * CPR; idx += 512) {
      int row = idx / CPR, c = idx - row * CPR;
      u32x4 v = *(LAS u32x4*)(uintptr_t)(otile + row * OST + c * 16);
      *(u32x4*)(dstm + (size_t)row * ldo + c * 8) = v;
    }
  }
}

__global__ __launch_bounds__(S1_THREADS, 3) void stage1_kernel(Params p) {
  extern __shared__ __attribute__((aligned(16))) char shm[];
  unsigned lds0 = (unsigned)(uintptr_t)(LAS char*)shm;
  int b = BIDX, xcd = b & 7, j = b >> 3;
  int rg = (xcd & 1) * 8 + (j & 7);
  int cq = xcd >> 1, ci = j >> 3;
  if (b == 0) {
    for (int i = TIDX; i < 64 * 32; i += S1_THREADS) ((unsigned*)(p.ws + WS_FLAGS))[i] = 0u;
  }
  if (ci < 2) stage1_body<0>(p, rg, cq * 2 + ci, lds0, xcd, j);
  else if (ci == 2) stage1_body<1>(p, rg, cq, lds0, xcd, j);
  else stage1_body<2>(p, rg, cq, lds0, xcd, j);
}

constexpr int K2_SLOT = 8192 + 12288;
constexpr int K2_NS = 7;
constexpr int K2_SCR = K2_NS * K2_SLOT;
constexpr int K2_LDS = K2_SCR + 20480;
constexpr int K2_THREADS = 1024;
constexpr int T_G = 10, T_A = 42, T_B = 52, T_END = 64;
constexpr unsigned SPIN_CAP = 1u << 22;

typedef __attribute__((address_space(1))) unsigned gu32;
DI void* uni_ptr(const void* q) {
  unsigned long long a = (unsigned long long)q;
  unsigned lo = __builtin_amdgcn_readfirstlane((unsigned)a), hi = __builtin_amdgcn_readfirstlane((unsigned)(a >> 32));
  return (void*)(((unsigned long long)hi << 32) | lo);
}
DI void store16_sc1(void* sbase, unsigned voff, u32x4 v) {
  sbase = uni_ptr(sbase);
  asm volatile("s_nop 4\n\tglobal_store_dwordx4 %0, %1, %2 sc1\n\ts_nop 1" ::"v"(voff), "v"(v), "s"(sbase) : "memory");
}
DI void glds16_sc1(const void* sbase, unsigned voff, unsigned lds_addr) {
  unsigned sv;
  asm volatile("s_mov_b32 %0, m0\n\ts_mov_b32 m0, %3\n\ts_nop 0\n\tglobal_load_lds_dwordx4 %1, %2 sc1\n\ts_mov_b32 m0, %0"
               : "=&s"(sv) : "v"(voff), "s"(sbase), "s"(lds_addr));
}
DI void wait_flags(unsigned* flags, int nw, unsigned target, int lane) {
  gu32* fp = (gu32*)(flags + (lane < nw ? lane : 0) * 32);
  for (unsigned spins = 0; spins < SPIN_CAP; ++spins) {
    unsigned v = __hip_atomic_load(fp, __ATOMIC_RELAXED, __HIP_MEMORY_SCOPE_AGENT);
    if (__all(v >= target)) break;
    __builtin_amdgcn_s_sleep(1);
  }
  __builtin_amdgcn_fence(__ATOMIC_ACQUIRE, "wavefront");
}

__global__ __launch_bounds__(K2_THREADS, 4) void stage2_kernel(Params p) {
  extern __shared__ __attribute__((aligned(16))) char shm[];
  const unsigned lds0 = (unsigned)(uintptr_t)(LAS char*)shm;
  const int b = BIDX, xcd = b & 7, j = b >> 3;
  const int rg = (xcd & 3) * 8 + (j & 7);
  const int cb = (xcd >> 2) * 4 + (j >> 3);
  const int rg3 = xcd * 4 + (j & 3), cb3 = j >> 2;
  const int tid = TIDX, lane = tid & 63;
  const int wid = __builtin_amdgcn_readfirstlane(tid >> 6);
  const int fr = lane & 15, g = lane >> 4;
  unsigned* flags = (unsigned*)(p.ws + WS_FLAGS);
  constexpr int CPR = 12;

  if (wid >= 12) {
    const int st = tid - 768, sw = wid - 12;
    const int mcb = b & 7;
    const unsigned scr = lds0 + K2_SCR;
    LAS float* zt = (LAS float*)(uintptr_t)(scr + 18432);
#define SB(n) do { _Pragma("unroll 1") for (int i_ = 0; i_ < (n); ++i_) BARRIER(); } while (0)
#define FLAG_ADD_S(off) do { if (st == 0) __hip_atomic_fetch_add((gu32*)(flags + (off)), 1u, __ATOMIC_RELAXED, __HIP_MEMORY_SCOPE_AGENT); } while (0)
    u32x4 w0, w1;
    u32x2 qw[5];
    {
      const u16* wsrc = b < 128 ? p.wo + (size_t)(b >> 3) * 40 * D_ : p.w_out + (size_t)((b - 128) >> 3) * 48 * D_;
      const int nch = b < 128 ? 400 : 480;
      int i1 = st + 256 < nch ? st + 256 : 0;
      w0 = *(const u32x4*)(wsrc + (st / 10) * D_ + mcb * 80 + (st % 10) * 8);
      w1 = *(const u32x4*)(wsrc + (i1 / 10) * D_ + mcb * 80 + (i1 % 10) * 8);
      const u16* qsrc = (const u16*)(p.ws + WS_Q) + (size_t)((b >> 1) * 16 + (st >> 4)) * D_ + (b & 1) * 320 + (st & 15) * 20;
#pragma unroll
      for (int q = 0; q < 5; ++q) qw[q] = *(const u32x2*)(qsrc + q * 4);
    }
    SB(3);
    float pq[20];
#pragma unroll
    for (int q = 0; q < 3; ++q) {
      pq[q * 4] = phi_f(bf_lo(qw[q][0])); pq[q * 4 + 1] = phi_f(bf_hi(qw[q][0]));
      pq[q * 4 + 2] = phi_f(bf_lo(qw[q][1])); pq[q * 4 + 3] = phi_f(bf_hi(qw[q][1]));
    }
    SBAR();
    SB(1);
#pragma unroll
    for (int q = 3; q < 5; ++q) {
      pq[q * 4] = phi_f(bf_lo(qw[q][0])); pq[q * 4 + 1] = phi_f(bf_hi(qw[q][0]));
      pq[q * 4 + 2] = phi_f(bf_lo(qw[q][1])); pq[q * 4 + 3] = phi_f(bf_hi(qw[q][1]));
    }
    *(LAS u32x4*)(uintptr_t)(scr + 8192 + st * 16) = w0;
    if (st + 256 < (b < 128 ? 400 : 480)) *(LAS u32x4*)(uintptr_t)(scr + 8192 + (st + 256) * 16) = w1;
    if (b < 128) {
      *(LAS u32x4*)(uintptr_t)(scr + st * 16) = u32x4{0, 0, 0, 0};
      *(LAS u32x4*)(uintptr_t)(scr + 4096 + st * 16) = u32x4{0, 0, 0, 0};
      if (st < 240) *(LAS u32x4*)(uintptr_t)(scr + 8192 + 6400 + st * 16) = u32x4{0, 0, 0, 0};
    }
    WAIT_LGKM0();
    SB(3);
    if (sw == 0) wait_flags(flags, 16, 16u, lane);
    SB(1);
    u32x4 tb0, tb1;
    {
      unsigned o0 = (((b < 128 ? b >> 3 : 0) * 2 + (st < 200 ? st / 100 : 0)) * 420 + (st % 100) * 4) * 4;
      int i4 = (st < 80 ? st : 0) * 4;
      unsigned o1 = (((b & 1) * 16 + i4 / 20) * 420 + 400 + i4 % 20) * 4;
      asm volatile("global_load_dwordx4 %0, %1, %2 sc1" : "=v"(tb0) : "v"(o0), "s"(p.ws + WS_KVR));
      asm volatile("global_load_dwordx4 %0, %1, %2 sc1" : "=v"(tb1) : "v"(o1), "s"(p.ws + WS_KVR));
    }
    const int q4 = fr >> 2, pp = fr & 3;
    if (b >= 128) {
#pragma unroll
      for (int it = 0; it < 2; ++it) {
        int blk = (it * 4 + sw) * 4 + g;
        int bb = blk < 30 ? blk : 0;
        int kc = bb / 5, nf = bb - kc * 5;
        unsigned wa = scr + 8192 + (kc * 8 + q4) * 160 + nf * 32 + pp * 8;
        bf16x8 v = cat8(lds_tr1(wa), lds_tr1(wa + 4 * 160));
        if (blk < 30) store16_sc1(p.ws + WS_B3, ((mcb * 176 + 80 + ((b - 128) >> 3) * 6 + kc) * 80 + nf * 16 + fr) * 16, __builtin_bit_cast(u32x4, v));
      }
    }
    SB(5);
    asm volatile("s_waitcnt vmcnt(0)" : "+v"(tb0), "+v"(tb1) :: "memory");
    if (st < 80) *(LAS u32x4*)(zt + st * 4) = tb1;
    if (b < 128 && st < 200) {
      int hh = st / 100, w = (st - hh * 100) * 4, dl = w / 20, el = w - dl * 20;
      int d = hh * 20 + dl, e = hh * 20 + el;
      int row = (d >> 5) * 32 + ((d >> 2) & 1) * 16 + ((d >> 3) & 3) * 4 + (d & 3);
      f32x4 kf = __builtin_bit_cast(f32x4, tb0);
      unsigned a = scr + row * 128 + ((((e >> 3) ^ ((row >> 1) & 7)) << 4) | ((e & 4) << 1));
      *(LAS u32x2*)(uintptr_t)a = u32x2{pack_bf16(kf[0], kf[1]), pack_bf16(kf[2], kf[3])};
    }
    WAIT_LGKM0();
    SB(1);
    {
      float den = 0.f;
#pragma unroll
      for (int q = 0; q < 5; ++q) {
        f32x4 zz = *(LAS f32x4*)(zt + (st & 15) * 20 + q * 4);
        den += pq[q * 4] * zz[0] + pq[q * 4 + 1] * zz[1] + pq[q * 4 + 2] * zz[2] + pq[q * 4 + 3] * zz[3];
      }
      float inv = 1.f / (den + 1e-6f);
#pragma unroll
      for (int q = 0; q < 5; ++q)
        qw[q] = u32x2{pack_bf16(pq[q * 4] * inv, pq[q * 4 + 1] * inv), pack_bf16(pq[q * 4 + 2] * inv, pq[q * 4 + 3] * inv)};
    }
    SBAR();
    SB(1);
#pragma unroll
    for (int it = 0; it < 3; ++it) {
      if (b < 128) {
        int unit = sw + it * 4;
        int u = unit < 10 ? unit : 0;
        int db = u / 5, nf = u - db * 5;
        f32x4 d0 = {0, 0, 0, 0}, d1 = {0, 0, 0, 0};
#pragma unroll
        for (int ks = 0; ks < 2; ++ks) {
          unsigned wa = scr + 8192 + (ks * 32 + g * 8 + q4) * 160 + nf * 32 + pp * 8;
          bf16x8 bw = cat8(lds_tr1(wa), lds_tr1(wa + 4 * 160));
          unsigned aa = scr + (db * 32 + fr) * 128 + (((ks * 4 + g) ^ ((fr >> 1) & 7)) << 4);
          d0 = mfma16(lds_b128(aa), bw, d0);
          d1 = mfma16(lds_b128(aa + 2048), bw, d1);
        }
        int kc = db * 4 + g;
        if (unit < 10 && kc < 5) {
          u32x4 o = {pack_bf16(d0[0], d0[1]), pack_bf16(d0[2], d0[3]), pack_bf16(d1[0], d1[1]), pack_bf16(d1[2], d1[3])};
          store16_sc1(p.ws + WS_B3, ((mcb * 176 + (b >> 3) * 5 + kc) * 80 + nf * 16 + fr) * 16, o);
        }
      }
      WAIT_LGKM0();
      SB(1);
    }
    constexpr int PST = 640 + 16;
    {
      unsigned a = scr + (st >> 4) * PST + (st & 15) * 40;
#pragma unroll
      for (int q = 0; q < 5; ++q) *(LAS u32x2*)(uintptr_t)(a + q * 8) = qw[q];
    }
    WAIT_LGKM0();
    SB(1);
    {
      const int prg = b >> 3, r0 = ((b >> 1) & 3) * 16;
#pragma unroll
      for (int it = 0; it < 3; ++it) {
        int idx = st + it * 256;
        if (idx < 640) {
          int ktl = idx >> 7, row = (idx >> 3) & 15, cp = idx & 7;
          int c = cp ^ (((r0 + row) >> 1) & 7);
          u32x4 v = *(LAS u32x4*)(uintptr_t)(scr + row * PST + (ktl * 8 + c) * 16);
          store16_sc1(p.ws + WS_UA, ((prg * 22 + (b & 1) * 5 + ktl) * 64 + r0 + row) * 128 + cp * 16, v);
        }
      }
    }
    SB(5);
    WAIT_VM(0);
    SB(1);
    FLAG_ADD_S(512 + (b & 15) * 32);
    SB(12);
    if (sw == 0) wait_flags(flags + 512, 16, 16u, lane);
    unsigned ul[3], ug[3];
    {
      constexpr int OST = 96 * 2 + 16;
#pragma unroll
      for (int it = 0; it < 3; ++it) {
        int idx = st + it * 256;
        int row = idx / CPR, c = idx - row * CPR;
        int cg = cb * 12 + c;
        ul[it] = pinv(scr + row * OST + c * 16);
        ug[it] = pinv(((rg * 22 + 10 + (cg >> 3)) * 64 + row) * 128 + (((cg & 7) ^ ((row >> 1) & 7)) << 4));
      }
    }
    SB(7);
    {
      u32x4 v[3];
#pragma unroll
      for (int it = 0; it < 3; ++it) v[it] = *(LAS u32x4*)(uintptr_t)ul[it];
#pragma unroll
      for (int it = 0; it < 3; ++it) store16_sc1(p.ws + WS_UA, ug[it], v[it]);
    }
    SB(2);
    WAIT_VM(0);
    SB(1);
    FLAG_ADD_S(1024 + rg * 32);
    SB(6);
    if (sw == 0) wait_flags(flags + 1024 + rg3 * 32, 1, 8u, lane);
    SB(1);
  } else if (wid >= 8) {
    __builtin_amdgcn_s_setprio(3);
    const int lw = wid - 8, lt = tid - 512;
    unsigned aoff1, aoff2, boff1[3];
    {
      int row = lw * 8 + (lane >> 3), c = (lane & 7) ^ ((row >> 1) & 7);
      aoff1 = ((rg * 64 + row) * D_ + c * 8) * 2;
      aoff2 = ((rg * 64 + row) * S_ + c * 8) * 2;
    }
#pragma unroll
    for (int i = 0; i < 3; ++i) {
      int slot = i * 256 + lt;
      int k = slot / CPR;
      boff1[i] = (k * (2 * F_) + cb * 96 + bswz<false>(k, slot - k * CPR) * 8) * 2;
    }
    const unsigned boff2 = lt * 16;
    const u16* zpanel = (const u16*)(p.ws + WS_Z2N) + (size_t)cb * S_ * 96;
    const u16* bpanel = (const u16*)(p.ws + WS_B3) + (size_t)cb3 * KA_ * 80;
    const u16* ua = (const u16*)(p.ws + WS_UA) + (size_t)rg3 * 22 * 4096;
    const bool b3 = lw < 2;
    const unsigned lbase = lds0 + lw * 1024;
    auto ld_z1_a = [&](int t, int sl) {
#pragma unroll
      for (int i = 0; i < 2; ++i) glds16(p.x + t * 64 + i * 32 * D_, aoff1, lbase + sl * K2_SLOT + i * 4096);
    };
    auto ld_z1_b = [&](int t, int sl) {
#pragma unroll
      for (int i = 0; i < 3; ++i) glds16(p.w_in + (size_t)t * 64 * (2 * F_), boff1[i], lbase + sl * K2_SLOT + 8192 + i * 4096);
    };
    auto ld_g_a = [&](int t, int sl) {
#pragma unroll
      for (int i = 0; i < 2; ++i) glds16(p.w_sp + t * 64 + i * 32 * S_, aoff2, lbase + sl * K2_SLOT + i * 4096);
    };
    auto ld_g_b = [&](int t, int sl) {
#pragma unroll
      for (int i = 0; i < 3; ++i) glds16_sc1(zpanel + (size_t)t * 64 * 96 + i * 2048, boff2, lbase + sl * K2_SLOT + 8192 + i * 4096);
    };
    auto ld_o_a = [&](int t, int sl) {
#pragma unroll
      for (int i = 0; i < 2; ++i) glds16_sc1(ua + t * 4096 + i * 2048, boff2, lbase + sl * K2_SLOT + i * 4096);
    };
    auto ld_o_b = [&](int t, int sl) {
#pragma unroll
      for (int i = 0; i < 2; ++i) glds16_sc1(bpanel + (size_t)t * 64 * 80 + i * 2048, boff2, lbase + sl * K2_SLOT + 8192 + i * 4096);
      if (b3) glds16_sc1(bpanel + (size_t)t * 64 * 80 + 4096, boff2, lbase + sl * K2_SLOT + 8192 + 8192);
    };
    int sl = 0;
    auto nx = [&]() { sl = sl == K2_NS - 1 ? 0 : sl + 1; };
#pragma unroll 1
    for (int t = 0; t < 2; ++t) { ld_z1_a(t, sl); ld_z1_b(t, sl); nx(); }
    WAIT_VM(5); BARRIER();
#pragma unroll 1
    for (int t = 2; t < 4; ++t) { ld_z1_a(t, sl); ld_z1_b(t, sl); nx(); }
    WAIT_VM(10); BARRIER();
#pragma unroll 1
    for (int t = 4; t < 6; ++t) { ld_z1_a(t, sl); ld_z1_b(t, sl); nx(); }
#pragma unroll 1
    for (int t = 6; t < 10; ++t) { ld_z1_a(t, sl); ld_z1_b(t, sl); nx(); WAIT_VM(20); BARRIER(); }
    ld_g_a(0, 3); WAIT_VM(17); BARRIER();
    ld_g_a(1, 4); WAIT_VM(14); BARRIER();
    ld_g_b(0, 3); ld_g_b(1, 4);
    sl = 5;
#pragma unroll 1
    for (int t = 2; t < 32; ++t) {
      ld_g_a(t, sl); ld_g_b(t, sl); nx();
      if (t == 4) WAIT_VM(18); else WAIT_VM(20);
      BARRIER();
    }
    ld_o_a(0, sl); ld_o_b(0, sl); nx(); if (b3) WAIT_VM(20); else WAIT_VM(19); BARRIER();
    ld_o_a(1, sl); ld_o_b(1, sl); nx(); if (b3) WAIT_VM(20); else WAIT_VM(18); BARRIER();
    ld_o_a(2, sl); ld_o_b(2, sl); nx(); if (b3) WAIT_VM(20); else WAIT_VM(17); BARRIER();
#pragma unroll 1
    for (int t = 3; t < 10; ++t) {
      ld_o_a(t, sl); ld_o_b(t, sl); nx();
      if (b3) WAIT_VM(20); else WAIT_VM(16);
      BARRIER();
    }
    const int sl52 = sl;
    ld_o_b(10, sl); nx(); if (b3) WAIT_VM(18); else WAIT_VM(14); BARRIER();
    ld_o_b(11, sl); nx(); if (b3) WAIT_VM(16); else WAIT_VM(12); BARRIER();
    ld_o_b(12, sl); nx(); if (b3) WAIT_VM(14); else WAIT_VM(10); BARRIER();
    ld_o_b(13, sl); nx(); if (b3) WAIT_VM(12); else WAIT_VM(8); BARRIER();
    ld_o_b(14, sl); nx(); BARRIER();
    ld_o_b(15, sl); nx(); BARRIER();
    {
      int s2 = sl52;
#pragma unroll 1
      for (int t = 10; t < 16; ++t) { ld_o_a(t, s2); s2 = s2 == K2_NS - 1 ? 0 : s2 + 1; }
    }
    WAIT_VM(10); BARRIER();
    WAIT_VM(8); BARRIER();
    ld_o_a(16, sl); ld_o_b(16, sl); nx(); if (b3) WAIT_VM(11); else WAIT_VM(10); BARRIER();
    ld_o_a(17, sl); ld_o_b(17, sl); nx(); if (b3) WAIT_VM(14); else WAIT_VM(12); BARRIER();
    ld_o_a(18, sl); ld_o_b(18, sl); nx(); if (b3) WAIT_VM(17); else WAIT_VM(14); BARRIER();
#pragma unroll 1
    for (int t = 19; t < 22; ++t) {
      ld_o_a(t, sl); ld_o_b(t, sl); nx();
      if (b3) WAIT_VM(20); else WAIT_VM(16);
      BARRIER();
    }
    if (b3) WAIT_VM(15); else WAIT_VM(12); BARRIER();
    if (b3) WAIT_VM(10); else WAIT_VM(8); BARRIER();
    if (b3) WAIT_VM(5); else WAIT_VM(4); BARRIER();
    WAIT_VM(0); BARRIER();
    BARRIER();
    BARRIER();
  } else {
    const int wr = wid >> 1, wc = wid & 1;
    const int kh = wid >> 2, wm = wid & 3;
    const int mcb = b & 7;
    const unsigned scr = lds0 + K2_SCR;
    LAS float* sx = (LAS float*)(uintptr_t)scr;
    LAS float* cst = (LAS float*)(uintptr_t)(scr + 16384);
    LAS float* krp = (LAS float*)(uintptr_t)(scr + 16384);
    const int urow = wr * 16 + fr;

    constexpr int ZST = 1536 + 32;
    u32x2 yv[3];
    u32x4 tb0;
    {
      unsigned oy = ((b * 8 + wid) * F_ + lane * 4) * 2;
#pragma unroll
      for (int i = 0; i < 3; ++i) asm volatile("global_load_dwordx2 %0, %1, %2" : "=v"(yv[i]) : "v"(oy + i * 512), "s"(p.ws + WS_Y2));
      unsigned ok = (((tid >> 4) & 15) * 13440 + (b < 210 ? b : 0) * 64 + (tid & 15) * 4) * 4;
      asm volatile("global_load_dwordx4 %0, %1, %2" : "=v"(tb0) : "v"(ok), "s"(p.ws + WS_KVP));
    }
    auto parta_compute = [&]() {
      asm volatile("s_waitcnt vmcnt(0)" : "+v"(yv[0]), "+v"(yv[1]), "+v"(yv[2]), "+v"(tb0) :: "memory");
      float z[12];
#pragma unroll
      for (int i = 0; i < 3; ++i)
#pragma unroll
        for (int q = 0; q < 2; ++q) {
          unsigned w = pack_bf16(gelu_f(bf_lo(yv[i][q])), gelu_f(bf_hi(yv[i][q])));
          z[i * 4 + q * 2] = bf_lo(w);
          z[i * 4 + q * 2 + 1] = bf_hi(w);
        }
      float s = 0.f;
#pragma unroll
      for (int i = 0; i < 12; ++i) s += z[i];
      s = wave_sum(s);
      float mean = s * (1.f / F_), v2 = 0.f;
#pragma unroll
      for (int i = 0; i < 12; ++i) { z[i] -= mean; v2 = __builtin_fmaf(z[i], z[i], v2); }
      v2 = wave_sum(v2);
      float rstd = __builtin_amdgcn_rsqf(v2 * (1.f / F_) + 1e-5f);
#pragma unroll
      for (int i = 0; i < 3; ++i)
        *(LAS u32x2*)(uintptr_t)(scr + wid * ZST + (i * 256 + lane * 4) * 2) =
            u32x2{pack_bf16(z[i * 4] * rstd, z[i * 4 + 1] * rstd), pack_bf16(z[i * 4 + 2] * rstd, z[i * 4 + 3] * rstd)};
      if (wid < 4) *(LAS u32x4*)(krp + tid * 4) = tb0;
      WAIT_LGKM0();
    };
    auto parta_store = [&]() {
#pragma unroll
      for (int it = 0; it < 2; ++it) {
        int grp64 = it ? 8 + (wid & 3) : wid;
        int q = fr >> 2, pp = fr & 3;
        unsigned a = scr + q * ZST + (grp64 * 64 + g * 16 + pp * 4) * 2;
        bf16x8 v = cat8(lds_tr1(a), lds_tr1(a + 4 * ZST));
        int f = grp64 * 64 + lane, c = f / 96, n = f - c * 96;
        if (it == 0 || wid < 4) store16_sc1(p.ws + WS_Z2N, ((c * 256 + b) * 96 + n) * 16, __builtin_bit_cast(u32x4, v));
      }
      if (tid >= 448 && tid < 464 && b < 210) {
        int v = tid - 448;
        f32x4 t4 = {0, 0, 0, 0};
#pragma unroll
        for (int q = 0; q < 16; ++q) t4 += *(LAS f32x4*)(krp + q * 64 + v * 4);
        store16_sc1(p.ws + WS_KVR, (b * 64 + v * 4) * 4, __builtin_bit_cast(u32x4, t4));
      }
    };

    unsigned abase[2], baddr1[3], baddr2, abase3, baddr3;
    {
      int x7 = (fr >> 1) & 7;
#pragma unroll
      for (int ks = 0; ks < 2; ++ks) abase[ks] = lds0 + (wr * 16 + fr) * 128 + (((ks * 4 + g) ^ x7) << 4);
      int q = fr >> 2, pp = fr & 3;
#pragma unroll
      for (int n = 0; n < 3; ++n) {
        int k = 8 * g + q;
        baddr1[n] = lds0 + 8192 + (k * CPR + bswz<false>(k, 2 * (wc * 3 + n) + (pp >> 1))) * 16 + (pp & 1) * 8;
      }
      baddr2 = lds0 + 8192 + (g * 96 + wc * 48 + fr) * 16;
      abase3 = lds0 + (wm * 16 + fr) * 128 + (((kh * 4 + g) ^ x7) << 4);
      baddr3 = lds0 + 8192 + ((kh * 4 + g) * 80 + fr) * 16;
    }
    f32x4 acc1[3], acc2[3], cs[3], acc3[5];
#pragma unroll
    for (int n = 0; n < 3; ++n) { acc1[n] = f32x4{0, 0, 0, 0}; acc2[n] = f32x4{0, 0, 0, 0}; cs[n] = f32x4{0, 0, 0, 0}; }
#pragma unroll
    for (int n = 0; n < 5; ++n) acc3[n] = f32x4{0, 0, 0, 0};
    const short one = 0x3f80;
    bf16x8 ones = {one, one, one, one, one, one, one, one};
    asm volatile("" : "+v"(ones));

    bf16x8 fA[8], fB[8];
    unsigned slot = 0;
    int z1t = 0;
    auto adv = [&]() { slot = slot == (K2_NS - 1) * K2_SLOT ? 0 : slot + K2_SLOT; };
    auto ldf = [&](bf16x8 (&f)[8], int mode, int i, unsigned& tmp) {
      if (mode == 2) {
        if (i == 0) f[0] = lds_b128(pinv(abase3 + slot));
        else if (i < 6) { if (i == 1) tmp = pinv(baddr3 + slot); f[1 + i] = lds_b128(tmp + (i - 1) * 256); }
      } else {
        if (i == 0 || i == 4) f[i >> 2] = lds_b128(pinv(abase[i >> 2] + slot));
        else {
          int ks = i >> 2, n = (i & 3) - 1;
          if (mode == 0) {
            unsigned a = pinv(baddr1[n] + slot) + ks * 32 * CPR * 16;
            f[2 + ks * 3 + n] = cat8(lds_tr1(a), lds_tr1(a + 4 * CPR * 16));
          } else {
            if (i == 1) tmp = pinv(baddr2 + slot);
            f[2 + ks * 3 + n] = lds_b128(tmp + ks * 4 * 96 * 16 + n * 256);
          }
        }
      }
    };
    auto iter = [&](bf16x8 (&fc)[8], bf16x8 (&fn)[8], auto mc, auto mn) {
      constexpr int MC = decltype(mc)::value, MN = decltype(mn)::value;
      unsigned tmp = 0;
#pragma unroll
      for (int i = 0; i < 8; ++i) {
        if (MC == 2) {
          if (i < 5) acc3[i] = mfma16(fc[2 + i], fc[0], acc3[i]);
        } else if (i < 6) {
          int ks = i / 3, n = i % 3;
          if (MC == 0) acc1[n] = mfma16(fc[2 + i], fc[ks], acc1[n]);
          else acc2[n] = mfma16(fc[2 + i], fc[ks], acc2[n]);
        }
        if (MN < 3) ldf(fn, MN, i, tmp);
        SBAR();
      }
      if (MC == 0) {
        if (wr == (z1t & 3)) {
#pragma unroll
          for (int i = 0; i < 6; ++i) cs[i % 3] = mfma16(fc[2 + i], ones, cs[i % 3]);
        }
        ++z1t;
        SBAR();
      }
      if (MN < 3) adv();
    };
    using I0 = std::integral_constant<int, 0>;
    using I1 = std::integral_constant<int, 1>;
    using I2 = std::integral_constant<int, 2>;
    using I3 = std::integral_constant<int, 3>;
#define FLAG_ADD(off) do { if (tid == 0) __hip_atomic_fetch_add((gu32*)(flags + (off)), 1u, __ATOMIC_RELAXED, __HIP_MEMORY_SCOPE_AGENT); } while (0)
    const f32x2 xs = *(const f32x2*)((const float*)(p.ws + WS_XST) + (size_t)(rg * 64 + urow) * 2);
    const float bias = bf_lo(p.b_sp[rg * 64 + urow]);
    parta_compute();
    BARRIER();
    parta_store();
    {
      unsigned tmp = 0;
#pragma unroll
      for (int i = 0; i < 8; ++i) ldf(fA, 0, i, tmp);
      adv();
    }
    BARRIER();
    iter(fA, fB, I0{}, I0{}); WAIT_VM(0); BARRIER();
    FLAG_ADD((b & 15) * 32);
    iter(fB, fA, I0{}, I0{}); BARRIER();
    iter(fA, fB, I0{}, I0{}); BARRIER();
    iter(fB, fA, I0{}, I0{}); BARRIER();
    iter(fA, fB, I0{}, I0{}); BARRIER();
    iter(fB, fA, I0{}, I0{}); BARRIER();
    iter(fA, fB, I0{}, I0{}); BARRIER();
    iter(fB, fA, I0{}, I0{}); BARRIER();
    iter(fA, fB, I0{}, I0{}); BARRIER();
    iter(fB, fA, I0{}, I1{}); BARRIER();
#pragma unroll 1
    for (int t = 10; t < 18; t += 2) {
      iter(fA, fB, I1{}, I1{}); BARRIER();
      iter(fB, fA, I1{}, I1{}); BARRIER();
    }
    iter(fA, fB, I1{}, I1{});
    if (fr == 0) {
#pragma unroll
      for (int n = 0; n < 3; ++n) *(LAS f32x4*)(cst + wr * 96 + wc * 48 + n * 16 + g * 4) = cs[n];
    }
    WAIT_LGKM0();
    BARRIER();
    auto z1act = [&](int n) {
      SBAR();
      int c0 = wc * 48 + n * 16 + g * 4;
      f32x4 c4 = (*(LAS f32x4*)(cst + c0) + *(LAS f32x4*)(cst + 96 + c0)) + (*(LAS f32x4*)(cst + 192 + c0) + *(LAS f32x4*)(cst + 288 + c0));
#pragma unroll
      for (int r = 0; r < 4; ++r) acc1[n][r] = gelu_f(__builtin_fmaf(acc1[n][r], xs[1], c4[r] * xs[0]));
      asm volatile("" : "+v"(acc1[n]));
      SBAR();
    };
    iter(fB, fA, I1{}, I1{}); z1act(0); BARRIER();
    iter(fA, fB, I1{}, I1{}); z1act(1); BARRIER();
    iter(fB, fA, I1{}, I1{}); z1act(2); BARRIER();
    iter(fA, fB, I1{}, I1{}); BARRIER();
    u32x4 xv0, xv1;
    unsigned go0, go1, la0, la1;
    {
      int i0 = tid, i1 = tid < 128 ? tid + 512 : tid;
      go0 = pinv((rg3 * 64 + i0 / 10) * D_ + cb3 * 80 + (i0 % 10) * 8);
      go1 = pinv((rg3 * 64 + i1 / 10) * D_ + cb3 * 80 + (i1 % 10) * 8);
      la0 = pinv(lds0 + (i0 / 10) * (80 * 4 + 16) + (i0 % 10) * 32);
      la1 = pinv(lds0 + (i1 / 10) * (80 * 4 + 16) + (i1 % 10) * 32);
      xv0 = *(const u32x4*)(p.x + go0);
      xv1 = *(const u32x4*)(p.x + go1);
    }
    iter(fB, fA, I1{}, I1{}); BARRIER();
#pragma unroll 1
    for (int t = 24; t < 34; t += 2) {
      iter(fA, fB, I1{}, I1{}); BARRIER();
      iter(fB, fA, I1{}, I1{}); BARRIER();
    }
    iter(fA, fB, I1{}, I1{}); BARRIER();
    iter(fB, fA, I1{}, I1{}); BARRIER();
#pragma unroll 1
    for (int t = 36; t < 40; t += 2) {
      iter(fA, fB, I1{}, I1{}); BARRIER();
      iter(fB, fA, I1{}, I1{}); BARRIER();
    }
    iter(fA, fB, I1{}, I1{}); BARRIER();
    iter(fB, fA, I1{}, I2{});
    constexpr int OST = 96 * 2 + 16;
    {
      unsigned dst = scr + urow * OST + (wc * 48 + g * 4) * 2;
#pragma unroll
      for (int n = 0; n < 3; ++n) {
        float v[4];
#pragma unroll
        for (int r = 0; r < 4; ++r) v[r] = acc1[n][r] * (acc2[n][r] + bias);
        *(LAS u32x2*)(uintptr_t)(dst + n * 32) = u32x2{pack_bf16(v[0], v[1]), pack_bf16(v[2], v[3])};
      }
    }
    SBAR();
    WAIT_LGKM0();
    BARRIER();
    iter(fA, fB, I2{}, I2{}); BARRIER();
    iter(fB, fA, I2{}, I2{}); BARRIER();
    iter(fA, fB, I2{}, I2{}); BARRIER();
    iter(fB, fA, I2{}, I2{}); BARRIER();
#pragma unroll 1
    for (int t = 46; t < 50; t += 2) {
      iter(fA, fB, I2{}, I2{}); BARRIER();
      iter(fB, fA, I2{}, I2{}); BARRIER();
    }
    iter(fA, fB, I2{}, I2{}); BARRIER();
    iter(fB, fA, I2{}, I3{});
    BARRIER();
    BARRIER();
    {
      unsigned tmp = 0;
#pragma unroll
      for (int i = 0; i < 8; ++i) ldf(fA, 2, i, tmp);
      adv();
    }
    BARRIER();
#pragma unroll 1
    for (int t = T_B; t < T_END; t += 2) {
      iter(fA, fB, I2{}, I2{}); BARRIER();
      iter(fB, fA, I2{}, I2{});
      if (t == T_END - 2) WAIT_LGKM0();
      BARRIER();
    }
    constexpr int OS3 = 80 * 4 + 16;
    {
      unsigned dst = lds0 + kh * (64 * OS3) + (wm * 16 + fr) * OS3 + g * 16;
#pragma unroll
      for (int n = 0; n < 5; ++n) *(LAS f32x4*)(uintptr_t)(dst + n * 64) = acc3[n];
    }
    WAIT_LGKM0();
    BARRIER();
#pragma unroll
    for (int it = 0; it < 2; ++it) {
      if (it == 0 || tid < 128) {
        u32x4 xv = it ? xv1 : xv0;
        unsigned la = it ? la1 : la0;
        f32x4 a = *(LAS f32x4*)(uintptr_t)(la) + *(LAS f32x4*)(uintptr_t)(la + 64 * OS3);
        f32x4 c4 = *(LAS f32x4*)(uintptr_t)(la + 16) + *(LAS f32x4*)(uintptr_t)(la + 64 * OS3 + 16);
        u32x4 o = {pack_bf16(bf_lo(xv[0]) + a[0], bf_hi(xv[0]) + a[1]), pack_bf16(bf_lo(xv[1]) + a[2], bf_hi(xv[1]) + a[3]),
                   pack_bf16(bf_lo(xv[2]) + c4[0], bf_hi(xv[2]) + c4[1]), pack_bf16(bf_lo(xv[3]) + c4[2], bf_hi(xv[3]) + c4[3])};
        *(u32x4*)(p.out + (it ? go1 : go0)) = o;
      }
    }
  }
}

extern "C" void kernel_launch(void* const* d_in, const int* in_sizes, int n_in, void* d_out, int out_size, void* d_ws,
                              size_t ws_size, hipStream_t stream) {
  Params p;
  p.x = (const u16*)d_in[0];
  p.wqkv = (const u16*)d_in[1];
  p.wo = (const u16*)d_in[2];
  p.w_in = (const u16*)d_in[4];
  p.w_sp = (const u16*)d_in[5];
  p.b_sp = (const u16*)d_in[6];
  p.w_out = (const u16*)d_in[7];
  p.out = (u16*)d_out;
  p.ws = (char*)d_ws;
  (void)hipFuncSetAttribute((const void*)stage1_kernel, hipFuncAttributeMaxDynamicSharedMemorySize, S1_LDS);
  (void)hipFuncSetAttribute((const void*)stage2_kernel, hipFuncAttributeMaxDynamicSharedMemorySize, K2_LDS);
  stage1_kernel<<<256, S1_THREADS, S1_LDS, stream>>>(p);
  stage2_kernel<<<256, K2_THREADS, K2_LDS, stream>>>(p);
}

#if defined(__HIP_DEVICE_COMPILE__) && __HIP_DEVICE_COMPILE__
#pragma clang attribute pop
#endif
```
